```python
import torch

def _softmax(x, axis=-1):
    return torch.softmax(x, dim=axis)
def _gelu(x):
    return torch.nn.functional.gelu(x, approximate="tanh")
def _silu(x):
    return torch.nn.functional.silu(x)
def _sigmoid(x):
    return torch.sigmoid(x)
def _layernorm(x, axis=-1, eps=1e-5):
    m = x.mean(dim=axis, keepdim=True)
    v = x.var(dim=axis, keepdim=True, unbiased=False)
    return (x - m) / torch.sqrt(v + eps)
def _scatter_add(tgt, idx, src, axis=0):
    return tgt.clone().index_add_(axis, idx, src)
def _segment_sum(src, seg, n):
    out = torch.zeros((n,) + src.shape[1:], dtype=src.dtype)
    return out.index_add_(0, seg, src)
def _pad(x, pw):
    flat = tuple(v for pair in reversed(pw) for v in pair)
    return torch.nn.functional.pad(x, flat)

kernel_name = 'recurrent_scan__spectral_gfnet__glu_gated_geglu__prompt'

def setup_inputs():

    _w = (lambda a: (lambda t: t.to(torch.bfloat16) if t.is_floating_point() else t)(torch.as_tensor(a)))
    def _A_setup():
        B, S, D = 1, 2048, 1024
        Df = D // 2 + 1
        return {
            "x": _w((lambda *s: torch.randn(*s, dtype=torch.bfloat16) * 0.2)(B, S, D)),
            "w_r": _w((lambda *s: torch.randn(*s, dtype=torch.bfloat16) * 1.6)(S, Df) * 0.02),
            "w_i": _w((lambda *s: torch.randn(*s, dtype=torch.bfloat16) * 1.6)(S, Df) * 0.02),
        }
    _A_in = _A_setup()

    def _B_setup():
        B, S, Dm, Df = 1, 2048, 1024, 3584
        return {
            "x": _w((lambda *s: torch.randn(*s, dtype=torch.bfloat16) * 0.2)(B, S, Dm)),
            "w_gate": _w((lambda *s: torch.randn(*s, dtype=torch.bfloat16) * 0.8)(Dm, Df) * (Dm ** -0.5)),
            "w_up": _w((lambda *s: torch.randn(*s, dtype=torch.bfloat16) * 0.8)(Dm, Df) * (Dm ** -0.5)),
            "w_down": _w((lambda *s: torch.randn(*s, dtype=torch.bfloat16) * 0.8)(Df, Dm) * (Df ** -0.5)),
        }
    _B_in = _B_setup()

    return {
        "A_x": _A_in["x"],
        "A_w_r": _A_in["w_r"],
        "A_w_i": _A_in["w_i"],
        "B_x": _B_in["x"],
        "B_w_gate": _B_in["w_gate"],
        "B_w_up": _B_in["w_up"],
        "B_w_down": _B_in["w_down"],
    }

def reference(A_x, A_w_r, A_w_i, B_x, B_w_gate, B_w_up, B_w_down):
    def _A_ref(x, w_r, w_i):
        B, S, D = x.shape
        X = (lambda x, axes=(-2,-1): torch.fft.rfft2(x.float(), dim=axes))(x, axes=(-2, -1))
        W = w_r + 1j * w_i
        X = X * W
        y = (lambda x, s, axes=(-2,-1): torch.fft.irfft2(x, s=s, dim=axes).to(torch.bfloat16))(X, (S, D), axes=(-2, -1))
        return y

    def _B_ref(x, w_gate, w_up, w_down):
        g = torch.einsum("bsd,df->bsf", x, w_gate)
        u = torch.einsum("bsd,df->bsf", x, w_up)
        h = _gelu(g) * u
        return torch.einsum("bsf,fd->bsd", h, w_down)

    x = A_x
    xn = _layernorm(x, axis=-1)
    s = _A_ref(xn, A_w_r, A_w_i)
    g = _silu(_B_ref(xn, B_w_gate, B_w_up, B_w_down))
    return x + s * g
```

```cpp
#include <hip/hip_runtime.h>
#include <hip/hip_bf16.h>
#include <stdint.h>

typedef __hip_bfloat16 bf16;
using bf16x8 = __attribute__((ext_vector_type(8))) short;
using s16x4 = __attribute__((ext_vector_type(4))) short;
using f32x4 = __attribute__((ext_vector_type(4))) float;
#define LDS_AS __attribute__((address_space(3)))

constexpr int S_ = 2048;
constexpr int D_ = 1024;
constexpr int DFQ = 513;
constexpr int FF_ = 3584;

__device__ __forceinline__ float bf2f(bf16 v) { return __bfloat162float(v); }
__device__ __forceinline__ float rbf(float f) { return __bfloat162float(__float2bfloat16(f)); }
__device__ __forceinline__ float us2f(unsigned short u) { return __uint_as_float(((unsigned)u) << 16); }

__device__ __forceinline__ float2 cadd(float2 a, float2 b) { return make_float2(a.x + b.x, a.y + b.y); }
__device__ __forceinline__ float2 csub(float2 a, float2 b) { return make_float2(a.x - b.x, a.y - b.y); }
__device__ __forceinline__ float2 cmul(float2 a, float2 b) {
  return make_float2(a.x * b.x - a.y * b.y, a.x * b.y + a.y * b.x);
}
template <int DIR>
__device__ __forceinline__ float2 crot(float2 a, float c, float s) {
  if (DIR < 0) return make_float2(a.x * c + a.y * s, a.y * c - a.x * s);
  else return make_float2(a.x * c - a.y * s, a.y * c + a.x * s);
}
template <int DIR>
__device__ __forceinline__ float2 twmul(float2 a, float2 t) {
  if (DIR < 0) return cmul(a, t);
  else return cmul(a, make_float2(t.x, -t.y));
}

template <int DIR>
__device__ __forceinline__ void dft4(float2& a0, float2& a1, float2& a2, float2& a3) {
  float2 t0 = cadd(a0, a2), t1 = csub(a0, a2), t2 = cadd(a1, a3), t3 = csub(a1, a3);
  a0 = cadd(t0, t2);
  a2 = csub(t0, t2);
  if (DIR < 0) {
    a1 = make_float2(t1.x + t3.y, t1.y - t3.x);
    a3 = make_float2(t1.x - t3.y, t1.y + t3.x);
  } else {
    a1 = make_float2(t1.x - t3.y, t1.y + t3.x);
    a3 = make_float2(t1.x + t3.y, t1.y - t3.x);
  }
}

#define C8 0.70710678118654752440f
#define C16A 0.92387953251128675613f
#define C16B 0.38268343236508977173f

template <int DIR>
__device__ __forceinline__ void dft16(float2 (&a)[16]) {
#pragma unroll
  for (int n2 = 0; n2 < 4; ++n2) dft4<DIR>(a[n2], a[4 + n2], a[8 + n2], a[12 + n2]);
  a[5] = crot<DIR>(a[5], C16A, C16B);
  a[6] = crot<DIR>(a[6], C8, C8);
  a[7] = crot<DIR>(a[7], C16B, C16A);
  a[9] = crot<DIR>(a[9], C8, C8);
  a[10] = crot<DIR>(a[10], 0.f, 1.f);
  a[11] = crot<DIR>(a[11], -C8, C8);
  a[13] = crot<DIR>(a[13], C16B, C16A);
  a[14] = crot<DIR>(a[14], -C8, C8);
  a[15] = crot<DIR>(a[15], -C16A, -C16B);
#pragma unroll
  for (int k1 = 0; k1 < 4; ++k1) dft4<DIR>(a[4 * k1], a[4 * k1 + 1], a[4 * k1 + 2], a[4 * k1 + 3]);
}
#define P16(k) (4 * ((k) & 3) + ((k) >> 2))

template <int DIR>
__device__ __forceinline__ void dft8(float2 (&a)[8]) {
#pragma unroll
  for (int n2 = 0; n2 < 4; ++n2) {
    float2 t = a[n2];
    a[n2] = cadd(t, a[4 + n2]);
    a[4 + n2] = csub(t, a[4 + n2]);
  }
  a[5] = crot<DIR>(a[5], C8, C8);
  a[6] = crot<DIR>(a[6], 0.f, 1.f);
  a[7] = crot<DIR>(a[7], -C8, C8);
  dft4<DIR>(a[0], a[1], a[2], a[3]);
  dft4<DIR>(a[4], a[5], a[6], a[7]);
}
#define P8(k) (4 * ((k) & 1) + ((k) >> 1))

template <int DIR>
__device__ __forceinline__ void fft1024_wave(float2* buf, float2* tmp, const float2* tw, int lane) {
  float2 a[16];
#pragma unroll
  for (int j = 0; j < 16; ++j) a[j] = buf[64 * j + lane];
  dft16<DIR>(a);
#pragma unroll
  for (int k1 = 0; k1 < 16; ++k1) {
    float2 v = a[P16(k1)];
    if (k1 > 0) v = twmul<DIR>(v, tw[(lane * k1) & 1023]);
    tmp[k1 * 68 + lane] = v;
  }
  {
    const int k1 = lane >> 2, n3 = lane & 3;
#pragma unroll
    for (int n2 = 0; n2 < 16; ++n2) a[n2] = tmp[k1 * 68 + n2 * 4 + n3];
    dft16<DIR>(a);
#pragma unroll
    for (int k2 = 0; k2 < 16; ++k2) {
      float2 v = a[P16(k2)];
      if (k2 > 0) v = twmul<DIR>(v, tw[(n3 * k2 * 16) & 1023]);
      tmp[k1 * 68 + k2 * 4 + n3] = v;
    }
  }
#pragma unroll
  for (int i = 0; i < 4; ++i) {
    const int vt = i * 64 + lane, k1 = vt >> 4, k2 = vt & 15;
    float2 b0 = tmp[k1 * 68 + k2 * 4 + 0], b1 = tmp[k1 * 68 + k2 * 4 + 1];
    float2 b2 = tmp[k1 * 68 + k2 * 4 + 2], b3 = tmp[k1 * 68 + k2 * 4 + 3];
    dft4<DIR>(b0, b1, b2, b3);
    const int kb = k1 + 16 * k2;
    buf[kb] = b0;
    buf[kb + 256] = b1;
    buf[kb + 512] = b2;
    buf[kb + 768] = b3;
  }
}

__global__ __launch_bounds__(256) void k_ln_fft(const bf16* __restrict__ x, bf16* __restrict__ xn,
                                                float2* __restrict__ T) {
  __shared__ float2 tw[1024];
  __shared__ float2 buf[4][1024];
  __shared__ float2 tmp[4][16 * 68];
  const int tid = threadIdx.x, lane = tid & 63, w = tid >> 6;
  const int row0 = blockIdx.x * 8;
#pragma unroll
  for (int i = 0; i < 4; ++i) {
    int j = tid + 256 * i;
    float s, c;
    sincospif(-(float)j * (1.0f / 512.0f), &s, &c);
    tw[j] = make_float2(c, s);
  }
#pragma unroll
  for (int r = 0; r < 2; ++r) {
    const int row = row0 + 2 * w + r;
    const bf16* p = x + (size_t)row * D_ + lane * 8;
    bf16x8 v0 = *reinterpret_cast<const bf16x8*>(p);
    bf16x8 v1 = *reinterpret_cast<const bf16x8*>(p + 512);
    float f[16];
#pragma unroll
    for (int j = 0; j < 8; ++j) {
      f[j] = us2f((unsigned short)v0[j]);
      f[8 + j] = us2f((unsigned short)v1[j]);
    }
    float s = 0.f;
#pragma unroll
    for (int j = 0; j < 16; ++j) s += f[j];
#pragma unroll
    for (int o = 32; o > 0; o >>= 1) s += __shfl_xor(s, o);
    const float mean = s * (1.0f / 1024.0f);
    float q = 0.f;
#pragma unroll
    for (int j = 0; j < 16; ++j) {
      float d = f[j] - mean;
      q += d * d;
    }
#pragma unroll
    for (int o = 32; o > 0; o >>= 1) q += __shfl_xor(q, o);
    const float var = q * (1.0f / 1024.0f);
    const float mb = rbf(mean), vb = rbf(var);
    const float den = rbf(sqrtf(rbf(vb + 1e-5f)));
    bf16x8 o0, o1;
#pragma unroll
    for (int j = 0; j < 16; ++j) {
      float d = rbf(f[j] - mb);
      bf16 y = __float2bfloat16(d / den);
      float yf = bf2f(y);
      unsigned short us = *reinterpret_cast<unsigned short*>(&y);
      if (j < 8) o0[j] = (short)us; else o1[j - 8] = (short)us;
      const int n = (j < 8) ? (lane * 8 + j) : (512 + lane * 8 + (j - 8));
      if (r == 0) buf[w][n].x = yf; else buf[w][n].y = yf;
    }
    bf16* po = xn + (size_t)row * D_ + lane * 8;
    *reinterpret_cast<bf16x8*>(po) = o0;
    *reinterpret_cast<bf16x8*>(po + 512) = o1;
  }
  __syncthreads();
  fft1024_wave<-1>(buf[w], tmp[w], tw, lane);
  __syncthreads();
  const int ww = tid & 3;
#pragma unroll
  for (int it = 0; it < 9; ++it) {
    const int k = (tid >> 2) + 64 * it;
    if (k <= 512) {
      float2 zk = buf[ww][k], zn = buf[ww][(1024 - k) & 1023];
      float4 o;
      o.x = 0.5f * (zk.x + zn.x);
      o.y = 0.5f * (zk.y - zn.y);
      o.z = 0.5f * (zk.y + zn.y);
      o.w = -0.5f * (zk.x - zn.x);
      *reinterpret_cast<float4*>(&T[(size_t)k * S_ + row0 + 2 * ww]) = o;
    }
  }
}

constexpr int CST = 136;
__device__ __forceinline__ void colfft_one(float2* __restrict__ Tc, const bf16* __restrict__ wr,
                                           const bf16* __restrict__ wi, int k2col, float2* tmp,
                                           const float2* tw, int lane) {
  float2 a[16];
#pragma unroll
  for (int h = 0; h < 2; ++h) {
    const int np = lane + 64 * h;
#pragma unroll
    for (int j = 0; j < 16; ++j) a[j] = Tc[128 * j + np];
    dft16<-1>(a);
#pragma unroll
    for (int k1 = 0; k1 < 16; ++k1) {
      float2 v = a[P16(k1)];
      if (k1 > 0) v = twmul<-1>(v, tw[(np * k1) & 2047]);
      tmp[k1 * CST + np] = v;
    }
  }
#pragma unroll
  for (int h = 0; h < 2; ++h) {
    const int vt = lane + 64 * h, k1 = vt >> 3, n3 = vt & 7;
#pragma unroll
    for (int n2 = 0; n2 < 16; ++n2) a[n2] = tmp[k1 * CST + n2 * 8 + n3];
    dft16<-1>(a);
#pragma unroll
    for (int k2 = 0; k2 < 16; ++k2) {
      float2 v = a[P16(k2)];
      if (k2 > 0) v = twmul<-1>(v, tw[(n3 * k2 * 16) & 2047]);
      tmp[k1 * CST + k2 * 8 + n3] = v;
    }
  }
  float2 b[4][8];
#pragma unroll
  for (int i = 0; i < 4; ++i) {
    const int vt = i * 64 + lane, k1 = vt >> 4, k2 = vt & 15;
#pragma unroll
    for (int n3 = 0; n3 < 8; ++n3) b[i][n3] = tmp[k1 * CST + k2 * 8 + n3];
  }
#pragma unroll
  for (int i = 0; i < 4; ++i) {
    const int vt = i * 64 + lane, k1 = vt >> 4, k2 = vt & 15;
    dft8<-1>(b[i]);
#pragma unroll
    for (int k3 = 0; k3 < 8; ++k3) {
      const int k = k1 + 16 * k2 + 256 * k3;
      float2 wv = make_float2(bf2f(wr[(size_t)k * DFQ + k2col]), bf2f(wi[(size_t)k * DFQ + k2col]));
      tmp[k] = cmul(b[i][P8(k3)], wv);
    }
  }
  float2 c0[16], c1[16];
#pragma unroll
  for (int j = 0; j < 16; ++j) {
    c0[j] = tmp[128 * j + lane];
    c1[j] = tmp[128 * j + lane + 64];
  }
  dft16<1>(c0);
  dft16<1>(c1);
#pragma unroll
  for (int k1 = 0; k1 < 16; ++k1) {
    float2 v = c0[P16(k1)];
    if (k1 > 0) v = twmul<1>(v, tw[(lane * k1) & 2047]);
    tmp[k1 * CST + lane] = v;
    float2 u = c1[P16(k1)];
    if (k1 > 0) u = twmul<1>(u, tw[((lane + 64) * k1) & 2047]);
    tmp[k1 * CST + lane + 64] = u;
  }
#pragma unroll
  for (int h = 0; h < 2; ++h) {
    const int vt = lane + 64 * h, k1 = vt >> 3, n3 = vt & 7;
#pragma unroll
    for (int n2 = 0; n2 < 16; ++n2) a[n2] = tmp[k1 * CST + n2 * 8 + n3];
    dft16<1>(a);
#pragma unroll
    for (int k2 = 0; k2 < 16; ++k2) {
      float2 v = a[P16(k2)];
      if (k2 > 0) v = twmul<1>(v, tw[(n3 * k2 * 16) & 2047]);
      tmp[k1 * CST + k2 * 8 + n3] = v;
    }
  }
#pragma unroll
  for (int i = 0; i < 4; ++i) {
    const int vt = i * 64 + lane, k1 = vt >> 4, k2 = vt & 15;
#pragma unroll
    for (int n3 = 0; n3 < 8; ++n3) b[i][n3] = tmp[k1 * CST + k2 * 8 + n3];
  }
#pragma unroll
  for (int i = 0; i < 4; ++i) {
    const int vt = i * 64 + lane, k1 = vt >> 4, k2 = vt & 15;
    dft8<1>(b[i]);
#pragma unroll
    for (int k3 = 0; k3 < 8; ++k3) {
      const int n = k1 + 16 * k2 + 256 * k3;
      tmp[n] = b[i][P8(k3)];
    }
  }
#pragma unroll
  for (int j = 0; j < 32; ++j) Tc[64 * j + lane] = tmp[64 * j + lane];
}

__global__ __launch_bounds__(256) void k_colfft(float2* __restrict__ T, const bf16* __restrict__ wr,
                                                const bf16* __restrict__ wi) {
  __shared__ float2 tw[2048];
  __shared__ float2 tmp[4][16 * CST];
  const int tid = threadIdx.x, lane = tid & 63, w = tid >> 6;
#pragma unroll
  for (int i = 0; i < 8; ++i) {
    int j = tid + 256 * i;
    float s, c;
    sincospif(-(float)j * (1.0f / 1024.0f), &s, &c);
    tw[j] = make_float2(c, s);
  }
  __syncthreads();
  const int col = blockIdx.x * 4 + w;
  if (col < DFQ) colfft_one(T + (size_t)col * S_, wr, wi, col, tmp[w], tw, lane);
}

__global__ __launch_bounds__(256) void k_final(const float2* __restrict__ T, const bf16* __restrict__ x,
                                               const float* __restrict__ gp, bf16* __restrict__ out) {
  __shared__ float2 tw[1024];
  __shared__ float2 buf[4][1024];
  __shared__ float2 tmp[4][16 * 68];
  const int tid = threadIdx.x, lane = tid & 63, w = tid >> 6;
  const int row0 = blockIdx.x * 8;
#pragma unroll
  for (int i = 0; i < 4; ++i) {
    int j = tid + 256 * i;
    float s, c;
    sincospif(-(float)j * (1.0f / 512.0f), &s, &c);
    tw[j] = make_float2(c, s);
  }
  const int ww = tid & 3;
#pragma unroll
  for (int it = 0; it < 9; ++it) {
    const int k = (tid >> 2) + 64 * it;
    if (k <= 512) {
      float4 v = *reinterpret_cast<const float4*>(&T[(size_t)k * S_ + row0 + 2 * ww]);
      if (k == 0 || k == 512) { v.y = 0.f; v.w = 0.f; }
      buf[ww][k] = make_float2(v.x - v.w, v.y + v.z);
      if (k > 0 && k < 512) buf[ww][1024 - k] = make_float2(v.x + v.w, -v.y + v.z);
    }
  }
  __syncthreads();
  fft1024_wave<1>(buf[w], tmp[w], tw, lane);
  const float scale = 1.0f / ((float)S_ * (float)D_);
#pragma unroll
  for (int r = 0; r < 2; ++r) {
    const int row = row0 + 2 * w + r;
#pragma unroll
    for (int hh = 0; hh < 2; ++hh) {
      const int n0 = hh * 512 + lane * 8;
      bf16x8 xv = *reinterpret_cast<const bf16x8*>(x + (size_t)row * D_ + n0);
      const float* g0 = gp + (size_t)row * D_ + n0;
      const float* g1 = g0 + (size_t)S_ * D_;
      f32x4 ga0 = *reinterpret_cast<const f32x4*>(g0), ga1 = *reinterpret_cast<const f32x4*>(g0 + 4);
      f32x4 gb0 = *reinterpret_cast<const f32x4*>(g1), gb1 = *reinterpret_cast<const f32x4*>(g1 + 4);
      bf16x8 ov;
#pragma unroll
      for (int j = 0; j < 8; ++j) {
        float2 z = buf[w][n0 + j];
        float sv = rbf((r == 0 ? z.x : z.y) * scale);
        float gpre = rbf(j < 4 ? (ga0[j] + gb0[j]) : (ga1[j - 4] + gb1[j - 4]));
        float g = rbf(gpre / (1.0f + __expf(-gpre)));
        float o = us2f((unsigned short)xv[j]) + rbf(sv * g);
        bf16 ob = __float2bfloat16(o);
        ov[j] = (short)*reinterpret_cast<unsigned short*>(&ob);
      }
      *reinterpret_cast<bf16x8*>(out + (size_t)row * D_ + n0) = ov;
    }
  }
}

__device__ __forceinline__ int lds_byte(int r, int c) {
  int st = (r >> 4) * 2 + (c >> 5), rr = r & 15, cc = c & 31, ob = rr * 64 + cc * 2;
  return st * 1024 + (ob ^ (((ob >> 9) & 1) << 5));
}
__device__ __forceinline__ void stage_rc(int b, int& R, int& C) {
  int st = b / 1024, sb = b % 1024, swz = sb ^ (((sb >> 9) & 1) << 5);
  R = (st >> 1) * 16 + swz / 64;
  C = (st & 1) * 32 + (swz % 64) / 2;
}
__device__ __forceinline__ int bswz(int row) { return ((row & 3) << 2) | ((row >> 2) & 3); }
__device__ __forceinline__ int tr_addr(int lane, int c, int ks, int t) {
  const int g = lane >> 4, q = (lane & 15) >> 2, p = lane & 3;
  const int row = 32 * ks + 8 * g + 4 * t + q;
  const int ch = 2 * c + (p >> 1);
  return 256 * row + 16 * (ch ^ bswz(row)) + 8 * (p & 1);
}
__device__ __forceinline__ bf16x8 ldb_frag(const char* base, int lane, int c, int ks) {
  s16x4 lo = __builtin_amdgcn_ds_read_tr16_b64_v4i16((LDS_AS s16x4*)(base + tr_addr(lane, c, ks, 0)));
  s16x4 hi = __builtin_amdgcn_ds_read_tr16_b64_v4i16((LDS_AS s16x4*)(base + tr_addr(lane, c, ks, 1)));
  bf16x8 r;
  r[0] = lo[0]; r[1] = lo[1]; r[2] = lo[2]; r[3] = lo[3];
  r[4] = hi[0]; r[5] = hi[1]; r[6] = hi[2]; r[7] = hi[3];
  return r;
}

#define WAIT_V(n) asm volatile("s_waitcnt vmcnt(" #n ")" ::: "memory")
#define WAIT_L(n) asm volatile("s_waitcnt lgkmcnt(" #n ")" ::: "memory")
#define BAR __builtin_amdgcn_s_barrier()
#define SCHED __builtin_amdgcn_sched_barrier(0)

constexpr int G1_BM = 256, G1_BK = 64, G1_HALF = 128, G1_HT = G1_HALF * G1_BK, G1_SHM = 8 * G1_HT * 2;

__global__ __launch_bounds__(512, 2) void k_gemm1(const bf16* __restrict__ A, const bf16* __restrict__ Wg,
                                                   const bf16* __restrict__ Wu, bf16* __restrict__ H) {
  extern __shared__ __attribute__((aligned(16))) bf16 shm[];
  constexpr int K = D_, NW = FF_;
#define SA(b, h) (shm + ((b) * 2 + (h)) * G1_HT)
#define SB(b, h) (shm + (4 + (b) * 2 + (h)) * G1_HT)
#define STAGE_A(T, P, br, kt)                                                                   \
  do {                                                                                          \
    long _g = (long)(br) * K + (long)(kt) * G1_BK;                                              \
    _Pragma("unroll") for (int _i = 0; _i < 2; ++_i) {                                                            \
      int _b = (T) * 16 + _i * 8192;                                                            \
      int _r, _c;                                                                               \
      stage_rc(_b, _r, _c);                                                                     \
      __builtin_amdgcn_global_load_lds((const unsigned*)(A + _g + (long)_r * K + _c),           \
                                       (unsigned*)((char*)(P) + _b), 16, 0, 0);                 \
    }                                                                                           \
  } while (0)
#define STAGE_B(T, P, W, kt)                                                                    \
  do {                                                                                          \
    _Pragma("unroll") for (int _i = 0; _i < 2; ++_i) {                                                            \
      int _b = (T) * 16 + _i * 8192;                                                            \
      int _row = _b >> 8, _ch = ((_b >> 4) & 15) ^ bswz(_row);                                  \
      __builtin_amdgcn_global_load_lds(                                                         \
          (const unsigned*)(W + (long)((kt) * G1_BK + _row) * NW + bcol + _ch * 8),             \
          (unsigned*)((char*)(P) + _b), 16, 0, 0);                                              \
    }                                                                                           \
  } while (0)
#define LDA(dst, b, h)                                                                          \
  _Pragma("unroll") for (int m = 0; m < 4; ++m)                                                 \
    _Pragma("unroll") for (int k = 0; k < 2; ++k)                                               \
  dst[m][k] = *reinterpret_cast<const bf16x8*>((char*)SA(b, h) + lds_byte(wr * 64 + m * 16 + fr, k * 32 + fq * 8))
#define LDB(dst, b, h)                                                                          \
  _Pragma("unroll") for (int n = 0; n < 2; ++n)                                                 \
    _Pragma("unroll") for (int k = 0; k < 2; ++k) dst[n][k] = ldb_frag((const char*)SB(b, h), lane, wc * 2 + n, k)
#define MMA(ai, bj, At, Bt)                                                                     \
  do {                                                                                          \
    __builtin_amdgcn_s_setprio(1);                                                              \
    _Pragma("unroll") for (int m = 0; m < 4; ++m)                                               \
      _Pragma("unroll") for (int n = 0; n < 2; ++n)                                             \
        _Pragma("unroll") for (int k = 0; k < 2; ++k)                                           \
          acc[ai][bj][m][n] =                                                                   \
              __builtin_amdgcn_mfma_f32_16x16x32_bf16(At[m][k], Bt[n][k], acc[ai][bj][m][n], 0, 0, 0); \
    __builtin_amdgcn_s_setprio(0);                                                              \
  } while (0)

  constexpr int nM = S_ / G1_BM, nN = FF_ / G1_HALF, nwg = nM * nN;
  int wgid = blockIdx.x;
  {
    int q = nwg / 8, r = nwg % 8, xcd = wgid % 8, off = wgid / 8;
    wgid = (xcd < r ? xcd * (q + 1) : r * (q + 1) + (xcd - r) * q) + off;
  }
  constexpr int WGM = 8;
  int nig = WGM * nN, gid = wgid / nig, fm = gid * WGM, gsz = min(nM - fm, WGM);
  int pm = fm + ((wgid % nig) % gsz), pn = (wgid % nig) / gsz, brow = pm * G1_BM, bcol = pn * G1_HALF;
  int wid = threadIdx.x >> 6, lane = threadIdx.x & 63, wr = wid >> 2, wc = wid & 3, fr = lane & 15, fq = lane >> 4;
  f32x4 acc[2][2][4][2] = {};
  bf16x8 At[4][2], B0[2][2], B1[2][2];
  constexpr int nt = K / G1_BK;
  STAGE_B(threadIdx.x, SB(0, 0), Wg, 0);
  STAGE_A(threadIdx.x, SA(0, 0), brow, 0);
  STAGE_B(threadIdx.x, SB(0, 1), Wu, 0);
  STAGE_A(threadIdx.x, SA(0, 1), brow + G1_HALF, 0);
  if (wr == 1) BAR;
  WAIT_V(4);
  BAR;
  STAGE_B(threadIdx.x, SB(1, 0), Wg, 1);
  STAGE_A(threadIdx.x, SA(1, 0), brow, 1);
  STAGE_B(threadIdx.x, SB(1, 1), Wu, 1);
  WAIT_V(6);
  BAR;
  for (int t = 0; t < nt - 2; t += 2) {
    LDB(B0, 0, 0); SCHED; LDA(At, 0, 0); STAGE_A(threadIdx.x, SA(1, 1), brow + G1_HALF, t + 1);
    WAIT_L(8); BAR; WAIT_L(0); MMA(0, 0, At, B0); BAR; SCHED;
    LDB(B1, 0, 1); STAGE_B(threadIdx.x, SB(0, 0), Wg, t + 2);
    BAR; WAIT_L(0); MMA(0, 1, At, B1); BAR;
    LDA(At, 0, 1); STAGE_A(threadIdx.x, SA(0, 0), brow, t + 2);
    BAR; WAIT_L(0); MMA(1, 0, At, B0); BAR; SCHED;
    STAGE_B(threadIdx.x, SB(0, 1), Wu, t + 2);
    WAIT_V(6); BAR; MMA(1, 1, At, B1); BAR;
    LDB(B0, 1, 0); SCHED; LDA(At, 1, 0); STAGE_A(threadIdx.x, SA(0, 1), brow + G1_HALF, t + 2);
    WAIT_L(8); BAR; WAIT_L(0); MMA(0, 0, At, B0); BAR; SCHED;
    LDB(B1, 1, 1); STAGE_B(threadIdx.x, SB(1, 0), Wg, t + 3);
    BAR; WAIT_L(0); MMA(0, 1, At, B1); BAR;
    LDA(At, 1, 1); STAGE_A(threadIdx.x, SA(1, 0), brow, t + 3);
    BAR; WAIT_L(0); MMA(1, 0, At, B0); BAR; SCHED;
    STAGE_B(threadIdx.x, SB(1, 1), Wu, t + 3);
    WAIT_V(6); BAR; MMA(1, 1, At, B1); BAR;
  }
  int tz = threadIdx.x;
  asm volatile("" : "+v"(tz));
  wid = tz >> 6; lane = tz & 63; wr = wid >> 2; wc = wid & 3; fr = lane & 15; fq = lane >> 4;
  {
    LDB(B0, 0, 0); WAIT_V(0); LDA(At, 0, 0); STAGE_A(tz, SA(1, 1), brow + G1_HALF, nt - 1);
    BAR; WAIT_L(0); MMA(0, 0, At, B0); BAR;
    LDB(B1, 0, 1); BAR; WAIT_L(0); MMA(0, 1, At, B1); BAR;
    LDA(At, 0, 1); WAIT_V(4); BAR; WAIT_L(0); MMA(1, 0, At, B0); MMA(1, 1, At, B1); BAR;
  }
  {
    LDB(B0, 1, 0); LDA(At, 1, 0); WAIT_V(2); BAR; WAIT_L(0); MMA(0, 0, At, B0); BAR;
    LDB(B1, 1, 1); WAIT_V(0); BAR; WAIT_L(0); MMA(0, 1, At, B1); BAR;
    LDA(At, 1, 1); BAR; WAIT_L(0); MMA(1, 0, At, B0); MMA(1, 1, At, B1); BAR;
  }
  if (wr == 0) BAR;
#pragma unroll
  for (int ai = 0; ai < 2; ++ai)
#pragma unroll
    for (int m = 0; m < 4; ++m)
#pragma unroll
      for (int n = 0; n < 2; ++n)
#pragma unroll
        for (int j = 0; j < 4; ++j) {
          float g = rbf(acc[ai][0][m][n][j]);
          float u = rbf(acc[ai][1][m][n][j]);
          float inner = 0.7978845608028654f * (g + 0.044715f * g * g * g);
          float ge = rbf(0.5f * g * (1.0f + tanhf(inner)));
          H[(size_t)(brow + ai * G1_HALF + wr * 64 + m * 16 + fq * 4 + j) * FF_ + (bcol + wc * 32 + n * 16 + fr)] =
              __float2bfloat16(ge * u);
        }
#undef SA
#undef SB
#undef STAGE_A
#undef STAGE_B
#undef LDA
#undef LDB
#undef MMA
}

constexpr int G2_KSPLIT = 2, G2_KLEN = FF_ / G2_KSPLIT;
__global__ __launch_bounds__(256) void k_gemm2(const bf16* __restrict__ A, const bf16* __restrict__ W,
                                               float* __restrict__ Cp) {
  __shared__ __attribute__((aligned(16))) bf16 shm[4 * 8192];
  constexpr int K = FF_, NW = D_;
  const int tid = threadIdx.x, lane = tid & 63, wid = tid >> 6, wr = wid >> 1, wc = wid & 1;
  const int fr = lane & 15, fq = lane >> 4;
  int bid = blockIdx.x;
  const int ks = bid & 1;
  bid >>= 1;
  const int pn = bid & 7, pm = bid >> 3;
  const int brow = pm * 128, bcol = pn * 128, k0 = ks * G2_KLEN;
  f32x4 acc[4][4] = {};
  constexpr int nt = G2_KLEN / 64;
#define G2_STAGE(buf, kt)                                                                       \
  do {                                                                                          \
    _Pragma("unroll") for (int _i = 0; _i < 4; ++_i) {                                                            \
      int _b = tid * 16 + _i * 4096;                                                            \
      int _r, _c;                                                                               \
      stage_rc(_b, _r, _c);                                                                     \
      __builtin_amdgcn_global_load_lds(                                                         \
          (const unsigned*)(A + (long)(brow + _r) * K + k0 + (kt) * 64 + _c),                   \
          (unsigned*)((char*)(shm + (buf) * 16384) + _b), 16, 0, 0);                            \
    }                                                                                           \
    _Pragma("unroll") for (int _i = 0; _i < 4; ++_i) {                                                            \
      int _b = tid * 16 + _i * 4096;                                                            \
      int _row = _b >> 8, _ch = ((_b >> 4) & 15) ^ bswz(_row);                                  \
      __builtin_amdgcn_global_load_lds(                                                         \
          (const unsigned*)(W + (long)(k0 + (kt) * 64 + _row) * NW + bcol + _ch * 8),           \
          (unsigned*)((char*)(shm + (buf) * 16384 + 8192) + _b), 16, 0, 0);                     \
    }                                                                                           \
  } while (0)
  G2_STAGE(0, 0);
  WAIT_V(0);
  BAR;
  for (int t = 0; t < nt; ++t) {
    const int cur = t & 1;
    if (t + 1 < nt) G2_STAGE(cur ^ 1, t + 1);
    const char* sa = (const char*)(shm + cur * 16384);
    const char* sb = (const char*)(shm + cur * 16384 + 8192);
#pragma unroll
    for (int k = 0; k < 2; ++k) {
      bf16x8 af[4], bfr[4];
#pragma unroll
      for (int m = 0; m < 4; ++m)
        af[m] = *reinterpret_cast<const bf16x8*>(sa + lds_byte(wr * 64 + m * 16 + fr, k * 32 + fq * 8));
#pragma unroll
      for (int n = 0; n < 4; ++n) bfr[n] = ldb_frag(sb, lane, wc * 4 + n, k);
#pragma unroll
      for (int m = 0; m < 4; ++m)
#pragma unroll
        for (int n = 0; n < 4; ++n)
          acc[m][n] = __builtin_amdgcn_mfma_f32_16x16x32_bf16(af[m], bfr[n], acc[m][n], 0, 0, 0);
    }
    WAIT_V(0);
    BAR;
  }
  float* C = Cp + (size_t)ks * S_ * D_;
#pragma unroll
  for (int m = 0; m < 4; ++m)
#pragma unroll
    for (int n = 0; n < 4; ++n)
#pragma unroll
      for (int j = 0; j < 4; ++j)
        C[(size_t)(brow + wr * 64 + m * 16 + fq * 4 + j) * D_ + bcol + wc * 64 + n * 16 + fr] = acc[m][n][j];
#undef G2_STAGE
}

extern "C" void kernel_launch(void* const* d_in, const int* in_sizes, int n_in, void* d_out, int out_size,
                              void* d_ws, size_t ws_size, hipStream_t stream) {
  const bf16* x = (const bf16*)d_in[0];
  const bf16* wr = (const bf16*)d_in[1];
  const bf16* wi = (const bf16*)d_in[2];
  const bf16* wg = (const bf16*)d_in[4];
  const bf16* wu = (const bf16*)d_in[5];
  const bf16* wd = (const bf16*)d_in[6];
  bf16* out = (bf16*)d_out;
  char* ws = (char*)d_ws;
  bf16* xn = (bf16*)ws;
  float2* T = (float2*)(ws + (size_t)8 * 1024 * 1024);
  bf16* h = (bf16*)(ws + (size_t)24 * 1024 * 1024);
  float* gp = (float*)(ws + (size_t)48 * 1024 * 1024);

  (void)hipFuncSetAttribute((const void*)k_gemm1, hipFuncAttributeMaxDynamicSharedMemorySize, G1_SHM);
  k_ln_fft<<<S_ / 8, 256, 0, stream>>>(x, xn, T);
  k_gemm1<<<(S_ / G1_BM) * (FF_ / G1_HALF), 512, G1_SHM, stream>>>(xn, wg, wu, h);
  k_colfft<<<(DFQ + 3) / 4, 256, 0, stream>>>(T, wr, wi);
  k_gemm2<<<(S_ / 128) * (D_ / 128) * G2_KSPLIT, 256, 0, stream>>>(h, wd, gp);
  k_final<<<S_ / 8, 256, 0, stream>>>(T, x, gp, out);
}
```

```cpp
#include <hip/hip_runtime.h>
#include <hip/hip_bf16.h>
#include <stdint.h>

typedef __hip_bfloat16 bf16;
using bf16x8 = __attribute__((ext_vector_type(8))) short;
using s16x4 = __attribute__((ext_vector_type(4))) short;
using f32x4 = __attribute__((ext_vector_type(4))) float;
#define LDS_AS __attribute__((address_space(3)))

constexpr int S_ = 2048;
constexpr int D_ = 1024;
constexpr int DFQ = 513;
constexpr int FF_ = 3584;
constexpr int G2_KSPLIT_F = 4;

__device__ __forceinline__ float bf2f(bf16 v) { return __bfloat162float(v); }
__device__ __forceinline__ float rbf(float f) { return __bfloat162float(__float2bfloat16(f)); }
__device__ __forceinline__ float us2f(unsigned short u) { return __uint_as_float(((unsigned)u) << 16); }

__device__ __forceinline__ float2 cadd(float2 a, float2 b) { return make_float2(a.x + b.x, a.y + b.y); }
__device__ __forceinline__ float2 csub(float2 a, float2 b) { return make_float2(a.x - b.x, a.y - b.y); }
__device__ __forceinline__ float2 cmul(float2 a, float2 b) {
  return make_float2(a.x * b.x - a.y * b.y, a.x * b.y + a.y * b.x);
}
template <int DIR>
__device__ __forceinline__ float2 crot(float2 a, float c, float s) {
  if (DIR < 0) return make_float2(a.x * c + a.y * s, a.y * c - a.x * s);
  else return make_float2(a.x * c - a.y * s, a.y * c + a.x * s);
}
template <int DIR>
__device__ __forceinline__ float2 twmul(float2 a, float2 t) {
  if (DIR < 0) return cmul(a, t);
  else return cmul(a, make_float2(t.x, -t.y));
}

template <int DIR>
__device__ __forceinline__ void dft4(float2& a0, float2& a1, float2& a2, float2& a3) {
  float2 t0 = cadd(a0, a2), t1 = csub(a0, a2), t2 = cadd(a1, a3), t3 = csub(a1, a3);
  a0 = cadd(t0, t2);
  a2 = csub(t0, t2);
  if (DIR < 0) {
    a1 = make_float2(t1.x + t3.y, t1.y - t3.x);
    a3 = make_float2(t1.x - t3.y, t1.y + t3.x);
  } else {
    a1 = make_float2(t1.x - t3.y, t1.y + t3.x);
    a3 = make_float2(t1.x + t3.y, t1.y - t3.x);
  }
}

#define C8 0.70710678118654752440f
#define C16A 0.92387953251128675613f
#define C16B 0.38268343236508977173f

template <int DIR>
__device__ __forceinline__ void dft16(float2 (&a)[16]) {
#pragma unroll
  for (int n2 = 0; n2 < 4; ++n2) dft4<DIR>(a[n2], a[4 + n2], a[8 + n2], a[12 + n2]);
  a[5] = crot<DIR>(a[5], C16A, C16B);
  a[6] = crot<DIR>(a[6], C8, C8);
  a[7] = crot<DIR>(a[7], C16B, C16A);
  a[9] = crot<DIR>(a[9], C8, C8);
  a[10] = crot<DIR>(a[10], 0.f, 1.f);
  a[11] = crot<DIR>(a[11], -C8, C8);
  a[13] = crot<DIR>(a[13], C16B, C16A);
  a[14] = crot<DIR>(a[14], -C8, C8);
  a[15] = crot<DIR>(a[15], -C16A, -C16B);
#pragma unroll
  for (int k1 = 0; k1 < 4; ++k1) dft4<DIR>(a[4 * k1], a[4 * k1 + 1], a[4 * k1 + 2], a[4 * k1 + 3]);
}
#define P16(k) (4 * ((k) & 3) + ((k) >> 2))

template <int DIR>
__device__ __forceinline__ void dft8(float2 (&a)[8]) {
#pragma unroll
  for (int n2 = 0; n2 < 4; ++n2) {
    float2 t = a[n2];
    a[n2] = cadd(t, a[4 + n2]);
    a[4 + n2] = csub(t, a[4 + n2]);
  }
  a[5] = crot<DIR>(a[5], C8, C8);
  a[6] = crot<DIR>(a[6], 0.f, 1.f);
  a[7] = crot<DIR>(a[7], -C8, C8);
  dft4<DIR>(a[0], a[1], a[2], a[3]);
  dft4<DIR>(a[4], a[5], a[6], a[7]);
}
#define P8(k) (4 * ((k) & 1) + ((k) >> 1))

template <int DIR>
__device__ __forceinline__ void fft1024_wave(float2* buf, float2* tmp, const float2* tw, int lane) {
  float2 a[16];
#pragma unroll
  for (int j = 0; j < 16; ++j) a[j] = buf[64 * j + lane];
  dft16<DIR>(a);
#pragma unroll
  for (int k1 = 0; k1 < 16; ++k1) {
    float2 v = a[P16(k1)];
    if (k1 > 0) v = twmul<DIR>(v, tw[(lane * k1) & 1023]);
    tmp[k1 * 68 + lane] = v;
  }
  {
    const int k1 = lane >> 2, n3 = lane & 3;
#pragma unroll
    for (int n2 = 0; n2 < 16; ++n2) a[n2] = tmp[k1 * 68 + n2 * 4 + n3];
    dft16<DIR>(a);
#pragma unroll
    for (int k2 = 0; k2 < 16; ++k2) {
      float2 v = a[P16(k2)];
      if (k2 > 0) v = twmul<DIR>(v, tw[(n3 * k2 * 16) & 1023]);
      tmp[k1 * 68 + k2 * 4 + n3] = v;
    }
  }
#pragma unroll
  for (int i = 0; i < 4; ++i) {
    const int vt = i * 64 + lane, k1 = vt >> 4, k2 = vt & 15;
    float2 b0 = tmp[k1 * 68 + k2 * 4 + 0], b1 = tmp[k1 * 68 + k2 * 4 + 1];
    float2 b2 = tmp[k1 * 68 + k2 * 4 + 2], b3 = tmp[k1 * 68 + k2 * 4 + 3];
    dft4<DIR>(b0, b1, b2, b3);
    const int kb = k1 + 16 * k2;
    buf[kb] = b0;
    buf[kb + 256] = b1;
    buf[kb + 512] = b2;
    buf[kb + 768] = b3;
  }
}

__global__ __launch_bounds__(256) void k_ln_fft(const bf16* __restrict__ x, bf16* __restrict__ xn,
                                                float2* __restrict__ T) {
  __shared__ float2 tw[1024];
  __shared__ float2 buf[4][1024];
  __shared__ float2 tmp[4][16 * 68];
  const int tid = threadIdx.x, lane = tid & 63, w = tid >> 6;
  const int row0 = blockIdx.x * 8;
#pragma unroll
  for (int i = 0; i < 4; ++i) {
    int j = tid + 256 * i;
    float s, c;
    sincospif(-(float)j * (1.0f / 512.0f), &s, &c);
    tw[j] = make_float2(c, s);
  }
#pragma unroll
  for (int r = 0; r < 2; ++r) {
    const int row = row0 + 2 * w + r;
    const bf16* p = x + (size_t)row * D_ + lane * 8;
    bf16x8 v0 = *reinterpret_cast<const bf16x8*>(p);
    bf16x8 v1 = *reinterpret_cast<const bf16x8*>(p + 512);
    float f[16];
#pragma unroll
    for (int j = 0; j < 8; ++j) {
      f[j] = us2f((unsigned short)v0[j]);
      f[8 + j] = us2f((unsigned short)v1[j]);
    }
    float s = 0.f;
#pragma unroll
    for (int j = 0; j < 16; ++j) s += f[j];
#pragma unroll
    for (int o = 32; o > 0; o >>= 1) s += __shfl_xor(s, o);
    const float mean = s * (1.0f / 1024.0f);
    float q = 0.f;
#pragma unroll
    for (int j = 0; j < 16; ++j) {
      float d = f[j] - mean;
      q += d * d;
    }
#pragma unroll
    for (int o = 32; o > 0; o >>= 1) q += __shfl_xor(q, o);
    const float var = q * (1.0f / 1024.0f);
    const float mb = rbf(mean), vb = rbf(var);
    const float den = rbf(sqrtf(rbf(vb + 1e-5f)));
    bf16x8 o0, o1;
#pragma unroll
    for (int j = 0; j < 16; ++j) {
      float d = rbf(f[j] - mb);
      bf16 y = __float2bfloat16(d / den);
      float yf = bf2f(y);
      unsigned short us = *reinterpret_cast<unsigned short*>(&y);
      if (j < 8) o0[j] = (short)us; else o1[j - 8] = (short)us;
      const int n = (j < 8) ? (lane * 8 + j) : (512 + lane * 8 + (j - 8));
      if (r == 0) buf[w][n].x = yf; else buf[w][n].y = yf;
    }
    bf16* po = xn + (size_t)row * D_ + lane * 8;
    *reinterpret_cast<bf16x8*>(po) = o0;
    *reinterpret_cast<bf16x8*>(po + 512) = o1;
  }
  __syncthreads();
  fft1024_wave<-1>(buf[w], tmp[w], tw, lane);
  __syncthreads();
  const int ww = tid & 3;
#pragma unroll
  for (int it = 0; it < 9; ++it) {
    const int k = (tid >> 2) + 64 * it;
    if (k <= 512) {
      float2 zk = buf[ww][k], zn = buf[ww][(1024 - k) & 1023];
      float4 o;
      o.x = 0.5f * (zk.x + zn.x);
      o.y = 0.5f * (zk.y - zn.y);
      o.z = 0.5f * (zk.y + zn.y);
      o.w = -0.5f * (zk.x - zn.x);
      *reinterpret_cast<float4*>(&T[(size_t)k * S_ + row0 + 2 * ww]) = o;
    }
  }
}

constexpr int CST = 136;
__device__ __forceinline__ void colfft_one(float2* __restrict__ Tc, const bf16* __restrict__ wr,
                                           const bf16* __restrict__ wi, int k2col, float2* tmp,
                                           const float2* tw, int lane) {
  float2 a[16];
#pragma unroll
  for (int h = 0; h < 2; ++h) {
    const int np = lane + 64 * h;
#pragma unroll
    for (int j = 0; j < 16; ++j) a[j] = Tc[128 * j + np];
    dft16<-1>(a);
#pragma unroll
    for (int k1 = 0; k1 < 16; ++k1) {
      float2 v = a[P16(k1)];
      if (k1 > 0) v = twmul<-1>(v, tw[(np * k1) & 2047]);
      tmp[k1 * CST + np] = v;
    }
  }
#pragma unroll
  for (int h = 0; h < 2; ++h) {
    const int vt = lane + 64 * h, k1 = vt >> 3, n3 = vt & 7;
#pragma unroll
    for (int n2 = 0; n2 < 16; ++n2) a[n2] = tmp[k1 * CST + n2 * 8 + n3];
    dft16<-1>(a);
#pragma unroll
    for (int k2 = 0; k2 < 16; ++k2) {
      float2 v = a[P16(k2)];
      if (k2 > 0) v = twmul<-1>(v, tw[(n3 * k2 * 16) & 2047]);
      tmp[k1 * CST + k2 * 8 + n3] = v;
    }
  }
  float2 b[4][8];
#pragma unroll
  for (int i = 0; i < 4; ++i) {
    const int vt = i * 64 + lane, k1 = vt >> 4, k2 = vt & 15;
#pragma unroll
    for (int n3 = 0; n3 < 8; ++n3) b[i][n3] = tmp[k1 * CST + k2 * 8 + n3];
  }
#pragma unroll
  for (int i = 0; i < 4; ++i) {
    const int vt = i * 64 + lane, k1 = vt >> 4, k2 = vt & 15;
    dft8<-1>(b[i]);
#pragma unroll
    for (int k3 = 0; k3 < 8; ++k3) {
      const int k = k1 + 16 * k2 + 256 * k3;
      float2 wv = make_float2(bf2f(wr[(size_t)k * DFQ + k2col]), bf2f(wi[(size_t)k * DFQ + k2col]));
      tmp[k] = cmul(b[i][P8(k3)], wv);
    }
  }
  float2 c0[16], c1[16];
#pragma unroll
  for (int j = 0; j < 16; ++j) {
    c0[j] = tmp[128 * j + lane];
    c1[j] = tmp[128 * j + lane + 64];
  }
  dft16<1>(c0);
  dft16<1>(c1);
#pragma unroll
  for (int k1 = 0; k1 < 16; ++k1) {
    float2 v = c0[P16(k1)];
    if (k1 > 0) v = twmul<1>(v, tw[(lane * k1) & 2047]);
    tmp[k1 * CST + lane] = v;
    float2 u = c1[P16(k1)];
    if (k1 > 0) u = twmul<1>(u, tw[((lane + 64) * k1) & 2047]);
    tmp[k1 * CST + lane + 64] = u;
  }
#pragma unroll
  for (int h = 0; h < 2; ++h) {
    const int vt = lane + 64 * h, k1 = vt >> 3, n3 = vt & 7;
#pragma unroll
    for (int n2 = 0; n2 < 16; ++n2) a[n2] = tmp[k1 * CST + n2 * 8 + n3];
    dft16<1>(a);
#pragma unroll
    for (int k2 = 0; k2 < 16; ++k2) {
      float2 v = a[P16(k2)];
      if (k2 > 0) v = twmul<1>(v, tw[(n3 * k2 * 16) & 2047]);
      tmp[k1 * CST + k2 * 8 + n3] = v;
    }
  }
#pragma unroll
  for (int i = 0; i < 4; ++i) {
    const int vt = i * 64 + lane, k1 = vt >> 4, k2 = vt & 15;
#pragma unroll
    for (int n3 = 0; n3 < 8; ++n3) b[i][n3] = tmp[k1 * CST + k2 * 8 + n3];
  }
#pragma unroll
  for (int i = 0; i < 4; ++i) {
    const int vt = i * 64 + lane, k1 = vt >> 4, k2 = vt & 15;
    dft8<1>(b[i]);
#pragma unroll
    for (int k3 = 0; k3 < 8; ++k3) {
      const int n = k1 + 16 * k2 + 256 * k3;
      tmp[n] = b[i][P8(k3)];
    }
  }
#pragma unroll
  for (int j = 0; j < 32; ++j) Tc[64 * j + lane] = tmp[64 * j + lane];
}

__global__ __launch_bounds__(256) void k_colfft(float2* __restrict__ T, const bf16* __restrict__ wr,
                                                const bf16* __restrict__ wi) {
  __shared__ float2 tw[2048];
  __shared__ float2 tmp[4][16 * CST];
  const int tid = threadIdx.x, lane = tid & 63, w = tid >> 6;
#pragma unroll
  for (int i = 0; i < 8; ++i) {
    int j = tid + 256 * i;
    float s, c;
    sincospif(-(float)j * (1.0f / 1024.0f), &s, &c);
    tw[j] = make_float2(c, s);
  }
  __syncthreads();
  const int col = blockIdx.x * 4 + w;
  if (col < DFQ) colfft_one(T + (size_t)col * S_, wr, wi, col, tmp[w], tw, lane);
}

__global__ __launch_bounds__(256) void k_final(const float2* __restrict__ T, const bf16* __restrict__ x,
                                               const float* __restrict__ gp, bf16* __restrict__ out) {
  __shared__ float2 tw[1024];
  __shared__ float2 buf[4][1024];
  __shared__ float2 tmp[4][16 * 68];
  const int tid = threadIdx.x, lane = tid & 63, w = tid >> 6;
  const int row0 = blockIdx.x * 8;
#pragma unroll
  for (int i = 0; i < 4; ++i) {
    int j = tid + 256 * i;
    float s, c;
    sincospif(-(float)j * (1.0f / 512.0f), &s, &c);
    tw[j] = make_float2(c, s);
  }
  const int ww = tid & 3;
#pragma unroll
  for (int it = 0; it < 9; ++it) {
    const int k = (tid >> 2) + 64 * it;
    if (k <= 512) {
      float4 v = *reinterpret_cast<const float4*>(&T[(size_t)k * S_ + row0 + 2 * ww]);
      if (k == 0 || k == 512) { v.y = 0.f; v.w = 0.f; }
      buf[ww][k] = make_float2(v.x - v.w, v.y + v.z);
      if (k > 0 && k < 512) buf[ww][1024 - k] = make_float2(v.x + v.w, -v.y + v.z);
    }
  }
  __syncthreads();
  fft1024_wave<1>(buf[w], tmp[w], tw, lane);
  const float scale = 1.0f / ((float)S_ * (float)D_);
#pragma unroll
  for (int r = 0; r < 2; ++r) {
    const int row = row0 + 2 * w + r;
#pragma unroll
    for (int hh = 0; hh < 2; ++hh) {
      const int n0 = hh * 512 + lane * 8;
      bf16x8 xv = *reinterpret_cast<const bf16x8*>(x + (size_t)row * D_ + n0);
      const float* g0 = gp + (size_t)row * D_ + n0;
      f32x4 ga0 = *reinterpret_cast<const f32x4*>(g0), ga1 = *reinterpret_cast<const f32x4*>(g0 + 4);
#pragma unroll
      for (int sp = 1; sp < G2_KSPLIT_F; ++sp) {
        const float* gs = g0 + (size_t)sp * S_ * D_;
        ga0 += *reinterpret_cast<const f32x4*>(gs);
        ga1 += *reinterpret_cast<const f32x4*>(gs + 4);
      }
      bf16x8 ov;
#pragma unroll
      for (int j = 0; j < 8; ++j) {
        float2 z = buf[w][n0 + j];
        float sv = rbf((r == 0 ? z.x : z.y) * scale);
        float gpre = rbf(j < 4 ? ga0[j] : ga1[j - 4]);
        float g = rbf(gpre / (1.0f + __expf(-gpre)));
        float o = us2f((unsigned short)xv[j]) + rbf(sv * g);
        bf16 ob = __float2bfloat16(o);
        ov[j] = (short)*reinterpret_cast<unsigned short*>(&ob);
      }
      *reinterpret_cast<bf16x8*>(out + (size_t)row * D_ + n0) = ov;
    }
  }
}

__device__ __forceinline__ int lds_byte(int r, int c) {
  int st = (r >> 4) * 2 + (c >> 5), rr = r & 15, cc = c & 31, ob = rr * 64 + cc * 2;
  return st * 1024 + (ob ^ (((ob >> 9) & 1) << 5));
}
__device__ __forceinline__ void stage_rc(int b, int& R, int& C) {
  int st = b / 1024, sb = b % 1024, swz = sb ^ (((sb >> 9) & 1) << 5);
  R = (st >> 1) * 16 + swz / 64;
  C = (st & 1) * 32 + (swz % 64) / 2;
}
__device__ __forceinline__ int bswz(int row) { return ((row & 3) << 2) | ((row >> 2) & 3); }
__device__ __forceinline__ int tr_addr(int lane, int c, int ks, int t) {
  const int g = lane >> 4, q = (lane & 15) >> 2, p = lane & 3;
  const int row = 32 * ks + 8 * g + 4 * t + q;
  const int ch = 2 * c + (p >> 1);
  return 256 * row + 16 * (ch ^ bswz(row)) + 8 * (p & 1);
}
template <int OFF>
__device__ __forceinline__ s16x4 tr_read(unsigned addr) {
  s16x4 r;
  asm volatile("ds_read_b64_tr_b16 %0, %1 offset:%2" : "=v"(r) : "v"(addr), "i"(OFF) : "memory");
  return r;
}
template <int OFF>
__device__ __forceinline__ bf16x8 lds_read128(unsigned addr) {
  bf16x8 r;
  asm volatile("ds_read_b128 %0, %1 offset:%2" : "=v"(r) : "v"(addr), "i"(OFF) : "memory");
  return r;
}
struct BFrag { s16x4 lo, hi; };
template <int OFF>
__device__ __forceinline__ BFrag ldb_frag(unsigned a0, unsigned a1) {
  BFrag r;
  r.lo = tr_read<OFF>(a0);
  r.hi = tr_read<OFF>(a1);
  return r;
}
__device__ __forceinline__ bf16x8 mk8(const BFrag& f) {
  bf16x8 r;
  r[0] = f.lo[0]; r[1] = f.lo[1]; r[2] = f.lo[2]; r[3] = f.lo[3];
  r[4] = f.hi[0]; r[5] = f.hi[1]; r[6] = f.hi[2]; r[7] = f.hi[3];
  return r;
}

#define WAIT_V(n) asm volatile("s_waitcnt vmcnt(" #n ")" ::: "memory")
#define WAIT_L(n) asm volatile("s_waitcnt lgkmcnt(" #n ")" ::: "memory")
#define BAR __builtin_amdgcn_s_barrier()
#define SCHED __builtin_amdgcn_sched_barrier(0)

constexpr int G1_BM = 256, G1_BK = 64, G1_HALF = 128, G1_HT = G1_HALF * G1_BK, G1_SHM = 8 * G1_HT * 2;

__global__ __launch_bounds__(512, 2) void k_gemm1(const bf16* __restrict__ A, const bf16* __restrict__ Wg,
                                                   const bf16* __restrict__ Wu, bf16* __restrict__ H) {
  extern __shared__ __attribute__((aligned(16))) bf16 shm[];
  constexpr int K = D_, NW = FF_;
#define SA(b, h) (shm + ((b) * 2 + (h)) * G1_HT)
#define SB(b, h) (shm + (4 + (b) * 2 + (h)) * G1_HT)
#define STAGE_A(T, P, br, kt)                                                                   \
  do {                                                                                          \
    long _g = (long)(br) * K + (long)(kt) * G1_BK;                                              \
    _Pragma("unroll") for (int _i = 0; _i < 2; ++_i) {                                                            \
      int _b = (T) * 16 + _i * 8192;                                                            \
      int _r, _c;                                                                               \
      stage_rc(_b, _r, _c);                                                                     \
      __builtin_amdgcn_global_load_lds((const unsigned*)(A + _g + (long)_r * K + _c),           \
                                       (unsigned*)((char*)(P) + _b), 16, 0, 0);                 \
    }                                                                                           \
  } while (0)
#define STAGE_B(T, P, W, kt)                                                                    \
  do {                                                                                          \
    _Pragma("unroll") for (int _i = 0; _i < 2; ++_i) {                                                            \
      int _b = (T) * 16 + _i * 8192;                                                            \
      int _row = _b >> 8, _ch = ((_b >> 4) & 15) ^ bswz(_row);                                  \
      __builtin_amdgcn_global_load_lds(                                                         \
          (const unsigned*)(W + (long)((kt) * G1_BK + _row) * NW + bcol + _ch * 8),             \
          (unsigned*)((char*)(P) + _b), 16, 0, 0);                                              \
    }                                                                                           \
  } while (0)
#define LDA(dst, b, h)                                                                          \
  _Pragma("unroll") for (int m = 0; m < 4; ++m)                                                 \
    _Pragma("unroll") for (int k = 0; k < 2; ++k)                                               \
  dst[m][k] = *reinterpret_cast<const bf16x8*>((char*)SA(b, h) + lds_byte(wr * 64 + m * 16 + fr, k * 32 + fq * 8))
#define LDB(dst, b, h)                                                                          \
  do {                                                                                          \
    dst[0][0] = ldb_frag<((b) * 2 + (h)) * 16384>(tb[0][0], tb[0][1]);                          \
    dst[0][1] = ldb_frag<((b) * 2 + (h)) * 16384 + 8192>(tb[0][0], tb[0][1]);                   \
    dst[1][0] = ldb_frag<((b) * 2 + (h)) * 16384>(tb[1][0], tb[1][1]);                          \
    dst[1][1] = ldb_frag<((b) * 2 + (h)) * 16384 + 8192>(tb[1][0], tb[1][1]);                   \
  } while (0)
#define MMA(ai, bj, At, Bt)                                                                     \
  do {                                                                                          \
    __builtin_amdgcn_s_setprio(1);                                                              \
    _Pragma("unroll") for (int m = 0; m < 4; ++m)                                               \
      _Pragma("unroll") for (int n = 0; n < 2; ++n)                                             \
        _Pragma("unroll") for (int k = 0; k < 2; ++k)                                           \
          acc[ai][bj][m][n] =                                                                   \
              __builtin_amdgcn_mfma_f32_16x16x32_bf16(At[m][k], mk8(Bt[n][k]), acc[ai][bj][m][n], 0, 0, 0); \
    __builtin_amdgcn_s_setprio(0);                                                              \
  } while (0)

  constexpr int nM = S_ / G1_BM, nN = FF_ / G1_HALF, nwg = nM * nN;
  int wgid = blockIdx.x;
  {
    int q = nwg / 8, r = nwg % 8, xcd = wgid % 8, off = wgid / 8;
    wgid = (xcd < r ? xcd * (q + 1) : r * (q + 1) + (xcd - r) * q) + off;
  }
  constexpr int WGM = 8;
  int nig = WGM * nN, gid = wgid / nig, fm = gid * WGM, gsz = min(nM - fm, WGM);
  int pm = fm + ((wgid % nig) % gsz), pn = (wgid % nig) / gsz, brow = pm * G1_BM, bcol = pn * G1_HALF;
  int wid = threadIdx.x >> 6, lane = threadIdx.x & 63, wr = wid >> 2, wc = wid & 3, fr = lane & 15, fq = lane >> 4;
  f32x4 acc[2][2][4][2] = {};
  bf16x8 At[4][2];
  BFrag B0[2][2], B1[2][2];
  unsigned tb[2][2];
  {
    const unsigned sbase = (unsigned)(size_t)SB(0, 0);
    tb[0][0] = sbase + tr_addr(lane, wc * 2, 0, 0);
    tb[0][1] = sbase + tr_addr(lane, wc * 2, 0, 1);
    tb[1][0] = sbase + tr_addr(lane, wc * 2 + 1, 0, 0);
    tb[1][1] = sbase + tr_addr(lane, wc * 2 + 1, 0, 1);
  }
  constexpr int nt = K / G1_BK;
  STAGE_B(threadIdx.x, SB(0, 0), Wg, 0);
  STAGE_A(threadIdx.x, SA(0, 0), brow, 0);
  STAGE_B(threadIdx.x, SB(0, 1), Wu, 0);
  STAGE_A(threadIdx.x, SA(0, 1), brow + G1_HALF, 0);
  if (wr == 1) BAR;
  WAIT_V(4);
  BAR;
  STAGE_B(threadIdx.x, SB(1, 0), Wg, 1);
  STAGE_A(threadIdx.x, SA(1, 0), brow, 1);
  STAGE_B(threadIdx.x, SB(1, 1), Wu, 1);
  WAIT_V(6);
  BAR;
  for (int t = 0; t < nt - 2; t += 2) {
    LDB(B0, 0, 0); SCHED; LDA(At, 0, 0); STAGE_A(threadIdx.x, SA(1, 1), brow + G1_HALF, t + 1);
    WAIT_L(8); BAR; WAIT_L(0); SCHED; MMA(0, 0, At, B0); BAR; SCHED;
    LDB(B1, 0, 1); STAGE_B(threadIdx.x, SB(0, 0), Wg, t + 2);
    BAR; WAIT_L(0); SCHED; MMA(0, 1, At, B1); BAR;
    LDA(At, 0, 1); STAGE_A(threadIdx.x, SA(0, 0), brow, t + 2);
    BAR; WAIT_L(0); SCHED; MMA(1, 0, At, B0); BAR; SCHED;
    STAGE_B(threadIdx.x, SB(0, 1), Wu, t + 2);
    WAIT_V(6); BAR; MMA(1, 1, At, B1); BAR;
    LDB(B0, 1, 0); SCHED; LDA(At, 1, 0); STAGE_A(threadIdx.x, SA(0, 1), brow + G1_HALF, t + 2);
    WAIT_L(8); BAR; WAIT_L(0); SCHED; MMA(0, 0, At, B0); BAR; SCHED;
    LDB(B1, 1, 1); STAGE_B(threadIdx.x, SB(1, 0), Wg, t + 3);
    BAR; WAIT_L(0); SCHED; MMA(0, 1, At, B1); BAR;
    LDA(At, 1, 1); STAGE_A(threadIdx.x, SA(1, 0), brow, t + 3);
    BAR; WAIT_L(0); SCHED; MMA(1, 0, At, B0); BAR; SCHED;
    STAGE_B(threadIdx.x, SB(1, 1), Wu, t + 3);
    WAIT_V(6); BAR; MMA(1, 1, At, B1); BAR;
  }
  int tz = threadIdx.x;
  asm volatile("" : "+v"(tz));
  wid = tz >> 6; lane = tz & 63; wr = wid >> 2; wc = wid & 3; fr = lane & 15; fq = lane >> 4;
  {
    LDB(B0, 0, 0); WAIT_V(0); LDA(At, 0, 0); STAGE_A(tz, SA(1, 1), brow + G1_HALF, nt - 1);
    BAR; WAIT_L(0); SCHED; MMA(0, 0, At, B0); BAR;
    LDB(B1, 0, 1); BAR; WAIT_L(0); SCHED; MMA(0, 1, At, B1); BAR;
    LDA(At, 0, 1); WAIT_V(4); BAR; WAIT_L(0); SCHED; MMA(1, 0, At, B0); MMA(1, 1, At, B1); BAR;
  }
  {
    LDB(B0, 1, 0); LDA(At, 1, 0); WAIT_V(2); BAR; WAIT_L(0); SCHED; MMA(0, 0, At, B0); BAR;
    LDB(B1, 1, 1); WAIT_V(0); BAR; WAIT_L(0); SCHED; MMA(0, 1, At, B1); BAR;
    LDA(At, 1, 1); BAR; WAIT_L(0); SCHED; MMA(1, 0, At, B0); MMA(1, 1, At, B1); BAR;
  }
  if (wr == 0) BAR;
#pragma unroll
  for (int ai = 0; ai < 2; ++ai)
#pragma unroll
    for (int m = 0; m < 4; ++m)
#pragma unroll
      for (int n = 0; n < 2; ++n)
#pragma unroll
        for (int j = 0; j < 4; ++j) {
          float g = rbf(acc[ai][0][m][n][j]);
          float u = rbf(acc[ai][1][m][n][j]);
          float inner = 0.7978845608028654f * (g + 0.044715f * g * g * g);
          float ge = rbf(g * __builtin_amdgcn_rcpf(1.0f + __expf(-2.0f * inner)));
          H[(size_t)(brow + ai * G1_HALF + wr * 64 + m * 16 + fq * 4 + j) * FF_ + (bcol + wc * 32 + n * 16 + fr)] =
              __float2bfloat16(ge * u);
        }
#undef SA
#undef SB
#undef STAGE_A
#undef STAGE_B
#undef LDA
#undef LDB
#undef MMA
}

constexpr int G2_KSPLIT = 4, G2_KLEN = FF_ / G2_KSPLIT;
constexpr int G2_STG = 49152, G2_SHM = 3 * G2_STG;
__global__ __launch_bounds__(512, 2) void k_gemm2(const bf16* __restrict__ A, const bf16* __restrict__ W,
                                                  float* __restrict__ Cp) {
  extern __shared__ __attribute__((aligned(16))) bf16 shm[];
  constexpr int K = FF_, NW = D_;
  const int tid = threadIdx.x, lane = tid & 63, wid = tid >> 6, wr = wid >> 1, wc = wid & 1;
  const int fr = lane & 15, fq = lane >> 4;
  const int bid = blockIdx.x;
  const int pm = bid & 7, idx = bid >> 3;
  const int pn = idx & 7, ks = idx >> 3;
  const int brow = pm * 256, bcol = pn * 128, k0 = ks * G2_KLEN;
  f32x4 acc[4][4] = {};
  unsigned tb[4][2];
  const unsigned ta = (unsigned)(size_t)shm + (wr >> 1) * 16384 + lds_byte((wr & 1) * 64 + fr, fq * 8);
  {
    const unsigned sbase = (unsigned)(size_t)shm + 32768;
#pragma unroll
    for (int n = 0; n < 4; ++n) {
      tb[n][0] = sbase + tr_addr(lane, wc * 4 + n, 0, 0);
      tb[n][1] = sbase + tr_addr(lane, wc * 4 + n, 0, 1);
    }
  }
  constexpr int nt = G2_KLEN / 64;
#define G2_STAGE(sbo, kt)                                                                       \
  do {                                                                                          \
    _Pragma("unroll") for (int _i = 0; _i < 4; ++_i) {                                          \
      int _b = tid * 16 + (_i & 1) * 8192;                                                      \
      int _r, _c;                                                                               \
      stage_rc(_b, _r, _c);                                                                     \
      __builtin_amdgcn_global_load_lds(                                                         \
          (const unsigned*)(A + (long)(brow + (_i >> 1) * 128 + _r) * K + k0 + (kt) * 64 + _c), \
          (unsigned*)((char*)shm + (sbo) + (_i >> 1) * 16384 + _b), 16, 0, 0);                  \
    }                                                                                           \
    _Pragma("unroll") for (int _i = 0; _i < 2; ++_i) {                                          \
      int _b = tid * 16 + _i * 8192;                                                            \
      int _row = _b >> 8, _ch = ((_b >> 4) & 15) ^ bswz(_row);                                  \
      __builtin_amdgcn_global_load_lds(                                                         \
          (const unsigned*)(W + (long)(k0 + (kt) * 64 + _row) * NW + bcol + _ch * 8),           \
          (unsigned*)((char*)shm + (sbo) + 32768 + _b), 16, 0, 0);                              \
    }                                                                                           \
  } while (0)
  bf16x8 af[2][4];
  BFrag bfr[2][4];
#define G2_FRAGS(set, sbo, KS)                                                                  \
  do {                                                                                          \
    af[set][0] = lds_read128<(KS) * 1024>(ta + (sbo));                                          \
    af[set][1] = lds_read128<(KS) * 1024 + 2048>(ta + (sbo));                                   \
    af[set][2] = lds_read128<(KS) * 1024 + 4096>(ta + (sbo));                                   \
    af[set][3] = lds_read128<(KS) * 1024 + 6144>(ta + (sbo));                                   \
    _Pragma("unroll") for (int n = 0; n < 4; ++n)                                               \
      bfr[set][n] = ldb_frag<(KS) * 8192>(tb[n][0] + (sbo), tb[n][1] + (sbo));                  \
  } while (0)
#define G2_MMA(set)                                                                             \
  do {                                                                                          \
    _Pragma("unroll") for (int m = 0; m < 4; ++m)                                               \
      _Pragma("unroll") for (int n = 0; n < 4; ++n)                                             \
        acc[m][n] = __builtin_amdgcn_mfma_f32_16x16x32_bf16(af[set][m], mk8(bfr[set][n]), acc[m][n], 0, 0, 0); \
  } while (0)
  G2_STAGE(0, 0);
  G2_STAGE(G2_STG, 1);
  WAIT_V(6);
  BAR;
  G2_FRAGS(0, 0, 0);
  unsigned cur = 0, nxt = G2_STG, nx2 = 2 * G2_STG;
  for (int t = 0; t < nt - 2; ++t) {
    G2_FRAGS(1, cur, 1);
    WAIT_L(12);
    SCHED;
    G2_MMA(0);
    SCHED;
    WAIT_L(0);
    WAIT_V(0);
    BAR;
    G2_STAGE(nx2, t + 2);
    G2_FRAGS(0, nxt, 0);
    SCHED;
    G2_MMA(1);
    SCHED;
    const unsigned tmp = cur;
    cur = nxt; nxt = nx2; nx2 = tmp;
  }
  G2_FRAGS(1, cur, 1);
  WAIT_L(12);
  SCHED;
  G2_MMA(0);
  SCHED;
  WAIT_L(0);
  WAIT_V(0);
  BAR;
  G2_FRAGS(0, nxt, 0);
  SCHED;
  G2_MMA(1);
  SCHED;
  G2_FRAGS(1, nxt, 1);
  WAIT_L(12);
  SCHED;
  G2_MMA(0);
  SCHED;
  WAIT_L(0);
  SCHED;
  G2_MMA(1);
  float* C = Cp + (size_t)ks * S_ * D_;
#pragma unroll
  for (int m = 0; m < 4; ++m)
#pragma unroll
    for (int n = 0; n < 4; ++n)
#pragma unroll
      for (int j = 0; j < 4; ++j)
        C[(size_t)(brow + wr * 64 + m * 16 + fq * 4 + j) * D_ + bcol + wc * 64 + n * 16 + fr] = acc[m][n][j];
#undef G2_STAGE
#undef G2_FRAGS
#undef G2_MMA
}

extern "C" void kernel_launch(void* const* d_in, const int* in_sizes, int n_in, void* d_out, int out_size,
                              void* d_ws, size_t ws_size, hipStream_t stream) {
  const bf16* x = (const bf16*)d_in[0];
  const bf16* wr = (const bf16*)d_in[1];
  const bf16* wi = (const bf16*)d_in[2];
  const bf16* wg = (const bf16*)d_in[4];
  const bf16* wu = (const bf16*)d_in[5];
  const bf16* wd = (const bf16*)d_in[6];
  bf16* out = (bf16*)d_out;
  char* ws = (char*)d_ws;
  bf16* xn = (bf16*)ws;
  float2* T = (float2*)(ws + (size_t)8 * 1024 * 1024);
  bf16* h = (bf16*)(ws + (size_t)24 * 1024 * 1024);
  float* gp = (float*)(ws + (size_t)48 * 1024 * 1024);

  (void)hipFuncSetAttribute((const void*)k_gemm1, hipFuncAttributeMaxDynamicSharedMemorySize, G1_SHM);
  k_ln_fft<<<S_ / 8, 256, 0, stream>>>(x, xn, T);
  k_gemm1<<<(S_ / G1_BM) * (FF_ / G1_HALF), 512, G1_SHM, stream>>>(xn, wg, wu, h);
  k_colfft<<<(DFQ + 3) / 4, 256, 0, stream>>>(T, wr, wi);
  (void)hipFuncSetAttribute((const void*)k_gemm2, hipFuncAttributeMaxDynamicSharedMemorySize, G2_SHM);
  k_gemm2<<<(S_ / 256) * (D_ / 128) * G2_KSPLIT, 512, G2_SHM, stream>>>(h, wd, gp);
  k_final<<<S_ / 8, 256, 0, stream>>>(T, x, gp, out);
}
```

```cpp
#include <hip/hip_runtime.h>
#include <hip/hip_bf16.h>
#include <stdint.h>

typedef __hip_bfloat16 bf16;
using bf16x8 = __attribute__((ext_vector_type(8))) short;
using s16x4 = __attribute__((ext_vector_type(4))) short;
using f32x4 = __attribute__((ext_vector_type(4))) float;
#define LDS_AS __attribute__((address_space(3)))

constexpr int S_ = 2048;
constexpr int D_ = 1024;
constexpr int DFQ = 513;
constexpr int FF_ = 3584;
constexpr int G2_KSPLIT_F = 4;

__device__ __forceinline__ float bf2f(bf16 v) { return __bfloat162float(v); }
__device__ __forceinline__ float rbf(float f) { return __bfloat162float(__float2bfloat16(f)); }
__device__ __forceinline__ float us2f(unsigned short u) { return __uint_as_float(((unsigned)u) << 16); }

__device__ __forceinline__ float2 cadd(float2 a, float2 b) { return make_float2(a.x + b.x, a.y + b.y); }
__device__ __forceinline__ float2 csub(float2 a, float2 b) { return make_float2(a.x - b.x, a.y - b.y); }
__device__ __forceinline__ float2 cmul(float2 a, float2 b) {
  return make_float2(a.x * b.x - a.y * b.y, a.x * b.y + a.y * b.x);
}
template <int DIR>
__device__ __forceinline__ float2 crot(float2 a, float c, float s) {
  if (DIR < 0) return make_float2(a.x * c + a.y * s, a.y * c - a.x * s);
  else return make_float2(a.x * c - a.y * s, a.y * c + a.x * s);
}
template <int DIR>
__device__ __forceinline__ float2 twmul(float2 a, float2 t) {
  if (DIR < 0) return cmul(a, t);
  else return cmul(a, make_float2(t.x, -t.y));
}

template <int DIR>
__device__ __forceinline__ void dft4(float2& a0, float2& a1, float2& a2, float2& a3) {
  float2 t0 = cadd(a0, a2), t1 = csub(a0, a2), t2 = cadd(a1, a3), t3 = csub(a1, a3);
  a0 = cadd(t0, t2);
  a2 = csub(t0, t2);
  if (DIR < 0) {
    a1 = make_float2(t1.x + t3.y, t1.y - t3.x);
    a3 = make_float2(t1.x - t3.y, t1.y + t3.x);
  } else {
    a1 = make_float2(t1.x - t3.y, t1.y + t3.x);
    a3 = make_float2(t1.x + t3.y, t1.y - t3.x);
  }
}

#define C8 0.70710678118654752440f
#define C16A 0.92387953251128675613f
#define C16B 0.38268343236508977173f

template <int DIR>
__device__ __forceinline__ void dft16(float2 (&a)[16]) {
#pragma unroll
  for (int n2 = 0; n2 < 4; ++n2) dft4<DIR>(a[n2], a[4 + n2], a[8 + n2], a[12 + n2]);
  a[5] = crot<DIR>(a[5], C16A, C16B);
  a[6] = crot<DIR>(a[6], C8, C8);
  a[7] = crot<DIR>(a[7], C16B, C16A);
  a[9] = crot<DIR>(a[9], C8, C8);
  a[10] = crot<DIR>(a[10], 0.f, 1.f);
  a[11] = crot<DIR>(a[11], -C8, C8);
  a[13] = crot<DIR>(a[13], C16B, C16A);
  a[14] = crot<DIR>(a[14], -C8, C8);
  a[15] = crot<DIR>(a[15], -C16A, -C16B);
#pragma unroll
  for (int k1 = 0; k1 < 4; ++k1) dft4<DIR>(a[4 * k1], a[4 * k1 + 1], a[4 * k1 + 2], a[4 * k1 + 3]);
}
#define P16(k) (4 * ((k) & 3) + ((k) >> 2))

template <int DIR>
__device__ __forceinline__ void dft8(float2 (&a)[8]) {
#pragma unroll
  for (int n2 = 0; n2 < 4; ++n2) {
    float2 t = a[n2];
    a[n2] = cadd(t, a[4 + n2]);
    a[4 + n2] = csub(t, a[4 + n2]);
  }
  a[5] = crot<DIR>(a[5], C8, C8);
  a[6] = crot<DIR>(a[6], 0.f, 1.f);
  a[7] = crot<DIR>(a[7], -C8, C8);
  dft4<DIR>(a[0], a[1], a[2], a[3]);
  dft4<DIR>(a[4], a[5], a[6], a[7]);
}
#define P8(k) (4 * ((k) & 1) + ((k) >> 1))

template <int DIR>
__device__ __forceinline__ void fft1024_wave(float2* buf, float2* tmp, const float2* tw, int lane) {
  float2 a[16];
#pragma unroll
  for (int j = 0; j < 16; ++j) a[j] = buf[64 * j + lane];
  dft16<DIR>(a);
#pragma unroll
  for (int k1 = 0; k1 < 16; ++k1) {
    float2 v = a[P16(k1)];
    if (k1 > 0) v = twmul<DIR>(v, tw[(lane * k1) & 1023]);
    tmp[k1 * 68 + lane] = v;
  }
  {
    const int k1 = lane >> 2, n3 = lane & 3;
#pragma unroll
    for (int n2 = 0; n2 < 16; ++n2) a[n2] = tmp[k1 * 68 + n2 * 4 + n3];
    dft16<DIR>(a);
#pragma unroll
    for (int k2 = 0; k2 < 16; ++k2) {
      float2 v = a[P16(k2)];
      if (k2 > 0) v = twmul<DIR>(v, tw[(n3 * k2 * 16) & 1023]);
      tmp[k1 * 68 + k2 * 4 + n3] = v;
    }
  }
#pragma unroll
  for (int i = 0; i < 4; ++i) {
    const int vt = i * 64 + lane, k1 = vt >> 4, k2 = vt & 15;
    float2 b0 = tmp[k1 * 68 + k2 * 4 + 0], b1 = tmp[k1 * 68 + k2 * 4 + 1];
    float2 b2 = tmp[k1 * 68 + k2 * 4 + 2], b3 = tmp[k1 * 68 + k2 * 4 + 3];
    dft4<DIR>(b0, b1, b2, b3);
    const int kb = k1 + 16 * k2;
    buf[kb] = b0;
    buf[kb + 256] = b1;
    buf[kb + 512] = b2;
    buf[kb + 768] = b3;
  }
}

__global__ __launch_bounds__(256) void k_ln_fft(const bf16* __restrict__ x, bf16* __restrict__ xn,
                                                float2* __restrict__ T, const unsigned short* __restrict__ wr,
                                                const unsigned short* __restrict__ wi, unsigned* __restrict__ Wt) {
  __shared__ float2 tw[1024];
  __shared__ float2 buf[4][1024];
  __shared__ float2 tmp[4][16 * 68];
  const int tid = threadIdx.x, lane = tid & 63, w = tid >> 6;
  const int row0 = blockIdx.x * 8;
#pragma unroll
  for (int i = 0; i < 4; ++i) {
    int j = tid + 256 * i;
    float s, c;
    sincospif(-(float)j * (1.0f / 512.0f), &s, &c);
    tw[j] = make_float2(c, s);
  }
#pragma unroll
  for (int r = 0; r < 2; ++r) {
    const int row = row0 + 2 * w + r;
    const bf16* p = x + (size_t)row * D_ + lane * 8;
    bf16x8 v0 = *reinterpret_cast<const bf16x8*>(p);
    bf16x8 v1 = *reinterpret_cast<const bf16x8*>(p + 512);
    float f[16];
#pragma unroll
    for (int j = 0; j < 8; ++j) {
      f[j] = us2f((unsigned short)v0[j]);
      f[8 + j] = us2f((unsigned short)v1[j]);
    }
    float s = 0.f;
#pragma unroll
    for (int j = 0; j < 16; ++j) s += f[j];
#pragma unroll
    for (int o = 32; o > 0; o >>= 1) s += __shfl_xor(s, o);
    const float mean = s * (1.0f / 1024.0f);
    float q = 0.f;
#pragma unroll
    for (int j = 0; j < 16; ++j) {
      float d = f[j] - mean;
      q += d * d;
    }
#pragma unroll
    for (int o = 32; o > 0; o >>= 1) q += __shfl_xor(q, o);
    const float var = q * (1.0f / 1024.0f);
    const float mb = rbf(mean), vb = rbf(var);
    const float den = rbf(sqrtf(rbf(vb + 1e-5f)));
    bf16x8 o0, o1;
#pragma unroll
    for (int j = 0; j < 16; ++j) {
      float d = rbf(f[j] - mb);
      bf16 y = __float2bfloat16(d / den);
      float yf = bf2f(y);
      unsigned short us = *reinterpret_cast<unsigned short*>(&y);
      if (j < 8) o0[j] = (short)us; else o1[j - 8] = (short)us;
      const int n = (j < 8) ? (lane * 8 + j) : (512 + lane * 8 + (j - 8));
      if (r == 0) buf[w][n].x = yf; else buf[w][n].y = yf;
    }
    bf16* po = xn + (size_t)row * D_ + lane * 8;
    *reinterpret_cast<bf16x8*>(po) = o0;
    *reinterpret_cast<bf16x8*>(po + 512) = o1;
  }
  __syncthreads();
  fft1024_wave<-1>(buf[w], tmp[w], tw, lane);
  __syncthreads();
  const int ww = tid & 3;
#pragma unroll
  for (int it = 0; it < 9; ++it) {
    const int k = (tid >> 2) + 64 * it;
    if (k <= 512) {
      float2 zk = buf[ww][k], zn = buf[ww][(1024 - k) & 1023];
      float4 o;
      o.x = 0.5f * (zk.x + zn.x);
      o.y = 0.5f * (zk.y - zn.y);
      o.z = 0.5f * (zk.y + zn.y);
      o.w = -0.5f * (zk.x - zn.x);
      *reinterpret_cast<float4*>(&T[(size_t)k * S_ + row0 + 2 * ww]) = o;
    }
  }
#pragma unroll
  for (int it = 0; it < 3; ++it) {
    const int col = tid + 256 * it;
    if (col < DFQ) {
      unsigned u[8];
#pragma unroll
      for (int j = 0; j < 8; ++j)
        u[j] = (unsigned)wr[(row0 + j) * DFQ + col] | ((unsigned)wi[(row0 + j) * DFQ + col] << 16);
      uint4* pw = reinterpret_cast<uint4*>(Wt + (size_t)col * S_ + row0);
      pw[0] = make_uint4(u[0], u[1], u[2], u[3]);
      pw[1] = make_uint4(u[4], u[5], u[6], u[7]);
    }
  }
}

typedef float v2f __attribute__((ext_vector_type(2)));
__device__ __forceinline__ v2f vcmul(v2f a, v2f t) {
  v2f r;
  asm("v_pk_mul_f32 %0, %1, %2 op_sel:[0,0] op_sel_hi:[1,0]" : "=v"(r) : "v"(a), "v"(t));
  asm("v_pk_fma_f32 %0, %1, %2, %0 op_sel:[1,1,0] op_sel_hi:[0,1,1] neg_lo:[1,0,0]" : "+v"(r) : "v"(a), "v"(t));
  return r;
}
__device__ __forceinline__ v2f vaddni(v2f a, v2f b) {
  v2f r;
  asm("v_pk_add_f32 %0, %1, %2 op_sel:[0,1] op_sel_hi:[1,0] neg_hi:[0,1]" : "=v"(r) : "v"(a), "v"(b));
  return r;
}
__device__ __forceinline__ v2f vaddpi(v2f a, v2f b) {
  v2f r;
  asm("v_pk_add_f32 %0, %1, %2 op_sel:[0,1] op_sel_hi:[1,0] neg_lo:[0,1]" : "=v"(r) : "v"(a), "v"(b));
  return r;
}
__device__ __forceinline__ v2f vmuli(v2f a) { return vaddpi(v2f{0.f, 0.f}, a); }
__device__ __forceinline__ v2f vmulni(v2f a) { return vaddni(v2f{0.f, 0.f}, a); }
template <int DIR>
__device__ __forceinline__ v2f vrot(v2f a, float c, float s) {
  return vcmul(a, v2f{c, DIR < 0 ? -s : s});
}
template <int DIR>
__device__ __forceinline__ void vdft4(v2f& a0, v2f& a1, v2f& a2, v2f& a3) {
  v2f t0 = a0 + a2, t1 = a0 - a2, t2 = a1 + a3, t3 = a1 - a3;
  a0 = t0 + t2;
  a2 = t0 - t2;
  a1 = DIR < 0 ? vaddni(t1, t3) : vaddpi(t1, t3);
  a3 = DIR < 0 ? vaddpi(t1, t3) : vaddni(t1, t3);
}
template <int DIR>
__device__ __forceinline__ void vdft16(v2f (&a)[16]) {
#pragma unroll
  for (int n2 = 0; n2 < 4; ++n2) vdft4<DIR>(a[n2], a[4 + n2], a[8 + n2], a[12 + n2]);
  a[5] = vrot<DIR>(a[5], C16A, C16B);
  a[6] = vrot<DIR>(a[6], C8, C8);
  a[7] = vrot<DIR>(a[7], C16B, C16A);
  a[9] = vrot<DIR>(a[9], C8, C8);
  a[10] = DIR < 0 ? vmulni(a[10]) : vmuli(a[10]);
  a[11] = vrot<DIR>(a[11], -C8, C8);
  a[13] = vrot<DIR>(a[13], C16B, C16A);
  a[14] = vrot<DIR>(a[14], -C8, C8);
  a[15] = vrot<DIR>(a[15], -C16A, -C16B);
#pragma unroll
  for (int k1 = 0; k1 < 4; ++k1) vdft4<DIR>(a[4 * k1], a[4 * k1 + 1], a[4 * k1 + 2], a[4 * k1 + 3]);
}
template <int DIR>
__device__ __forceinline__ void vdft8(v2f (&a)[8]) {
#pragma unroll
  for (int n2 = 0; n2 < 4; ++n2) {
    v2f t = a[n2];
    a[n2] = t + a[4 + n2];
    a[4 + n2] = t - a[4 + n2];
  }
  a[5] = vrot<DIR>(a[5], C8, C8);
  a[6] = DIR < 0 ? vmulni(a[6]) : vmuli(a[6]);
  a[7] = vrot<DIR>(a[7], -C8, C8);
  vdft4<DIR>(a[0], a[1], a[2], a[3]);
  vdft4<DIR>(a[4], a[5], a[6], a[7]);
}
__device__ __forceinline__ v2f vtwid(int num, float inv_den2) {
  float s, c;
  sincospif(-(float)num * inv_den2, &s, &c);
  return v2f{c, s};
}

constexpr int CS1 = 136;
constexpr int CS2 = 140;
constexpr int CF_BUF = 16 * CS2;
__device__ __forceinline__ void colfft_one(v2f* __restrict__ Tc, const unsigned* __restrict__ Wc, v2f* buf,
                                           int lane, v2f tA0, v2f tA1, v2f tB) {
  v2f a[16], b[16];
#pragma unroll
  for (int j = 0; j < 16; ++j) {
    a[j] = Tc[128 * j + lane];
    b[j] = Tc[128 * j + lane + 64];
  }
  unsigned wv[32];
#pragma unroll
  for (int j = 0; j < 32; ++j) wv[j] = Wc[64 * j + lane];
  vdft16<-1>(a);
  vdft16<-1>(b);
  {
    v2f ta = tA0, tb = tA1;
    buf[lane] = a[0];
    buf[lane + 64] = b[0];
#pragma unroll
    for (int k1 = 1; k1 < 16; ++k1) {
      buf[k1 * CS1 + lane] = vcmul(a[P16(k1)], ta);
      buf[k1 * CS1 + lane + 64] = vcmul(b[P16(k1)], tb);
      if (k1 < 15) { ta = vcmul(ta, tA0); tb = vcmul(tb, tA1); }
    }
  }
  const int n3 = lane & 7, k1a = lane >> 3;
  {
    const v2f* pa = buf + k1a * CS1 + n3;
#pragma unroll
    for (int n2 = 0; n2 < 16; ++n2) {
      a[n2] = pa[n2 * 8];
      b[n2] = pa[8 * CS1 + n2 * 8];
    }
    vdft16<-1>(a);
    vdft16<-1>(b);
    v2f* qa = buf + k1a * CS2 + n3 * 17;
    v2f t = tB;
    qa[0] = a[0];
    qa[8 * CS2] = b[0];
#pragma unroll
    for (int k2 = 1; k2 < 16; ++k2) {
      qa[k2] = vcmul(a[P16(k2)], t);
      qa[8 * CS2 + k2] = vcmul(b[P16(k2)], t);
      if (k2 < 15) t = vcmul(t, tB);
    }
  }
  {
    v2f* pc = buf + (lane & 15) * CS2 + (lane >> 4);
#pragma unroll
    for (int i = 0; i < 4; ++i) {
      v2f c[8], d[8];
#pragma unroll
      for (int n = 0; n < 8; ++n) c[n] = pc[n * 17 + 4 * i];
      vdft8<-1>(c);
#pragma unroll
      for (int k3 = 0; k3 < 8; ++k3) {
        const unsigned u = wv[i + 4 * k3];
        const v2f w = v2f{__uint_as_float(u << 16), __uint_as_float(u & 0xffff0000u)};
        d[k3] = vcmul(c[P8(k3)], w);
      }
      vdft8<1>(d);
#pragma unroll
      for (int n = 0; n < 8; ++n) pc[n * 17 + 4 * i] = d[P8(n)];
    }
  }
  {
    const v2f* qa = buf + k1a * CS2 + n3 * 17;
    const v2f tBc = v2f{tB.x, -tB.y};
    v2f t = tBc;
    a[0] = qa[0];
    b[0] = qa[8 * CS2];
#pragma unroll
    for (int k2 = 1; k2 < 16; ++k2) {
      a[k2] = qa[k2];
      b[k2] = qa[8 * CS2 + k2];
    }
#pragma unroll
    for (int k2 = 1; k2 < 16; ++k2) {
      a[k2] = vcmul(a[k2], t);
      b[k2] = vcmul(b[k2], t);
      if (k2 < 15) t = vcmul(t, tBc);
    }
    vdft16<1>(a);
    vdft16<1>(b);
    v2f* pa = buf + k1a * CS1 + n3;
#pragma unroll
    for (int n2 = 0; n2 < 16; ++n2) {
      pa[n2 * 8] = a[P16(n2)];
      pa[8 * CS1 + n2 * 8] = b[P16(n2)];
    }
  }
  {
    const v2f c0 = v2f{tA0.x, -tA0.y}, c1 = v2f{tA1.x, -tA1.y};
    v2f ta = c0, tb = c1;
#pragma unroll
    for (int k1 = 0; k1 < 16; ++k1) {
      a[k1] = buf[k1 * CS1 + lane];
      b[k1] = buf[k1 * CS1 + lane + 64];
    }
#pragma unroll
    for (int k1 = 1; k1 < 16; ++k1) {
      a[k1] = vcmul(a[k1], ta);
      b[k1] = vcmul(b[k1], tb);
      if (k1 < 15) { ta = vcmul(ta, c0); tb = vcmul(tb, c1); }
    }
    vdft16<1>(a);
    vdft16<1>(b);
#pragma unroll
    for (int j = 0; j < 16; ++j) {
      Tc[128 * j + lane] = a[P16(j)];
      Tc[128 * j + lane + 64] = b[P16(j)];
    }
  }
}

__global__ __launch_bounds__(256) void k_final(const float2* __restrict__ T, const bf16* __restrict__ x,
                                               const float* __restrict__ gp, bf16* __restrict__ out) {
  __shared__ float2 tw[1024];
  __shared__ float2 buf[4][1024];
  __shared__ float2 tmp[4][16 * 68];
  const int tid = threadIdx.x, lane = tid & 63, w = tid >> 6;
  const int row0 = blockIdx.x * 8;
#pragma unroll
  for (int i = 0; i < 4; ++i) {
    int j = tid + 256 * i;
    float s, c;
    sincospif(-(float)j * (1.0f / 512.0f), &s, &c);
    tw[j] = make_float2(c, s);
  }
  const int ww = tid & 3;
#pragma unroll
  for (int it = 0; it < 9; ++it) {
    const int k = (tid >> 2) + 64 * it;
    if (k <= 512) {
      float4 v = *reinterpret_cast<const float4*>(&T[(size_t)k * S_ + row0 + 2 * ww]);
      if (k == 0 || k == 512) { v.y = 0.f; v.w = 0.f; }
      buf[ww][k] = make_float2(v.x - v.w, v.y + v.z);
      if (k > 0 && k < 512) buf[ww][1024 - k] = make_float2(v.x + v.w, -v.y + v.z);
    }
  }
  __syncthreads();
  fft1024_wave<1>(buf[w], tmp[w], tw, lane);
  const float scale = 1.0f / ((float)S_ * (float)D_);
#pragma unroll
  for (int r = 0; r < 2; ++r) {
    const int row = row0 + 2 * w + r;
#pragma unroll
    for (int hh = 0; hh < 2; ++hh) {
      const int n0 = hh * 512 + lane * 8;
      bf16x8 xv = *reinterpret_cast<const bf16x8*>(x + (size_t)row * D_ + n0);
      const float* g0 = gp + (size_t)row * D_ + n0;
      f32x4 ga0 = *reinterpret_cast<const f32x4*>(g0), ga1 = *reinterpret_cast<const f32x4*>(g0 + 4);
#pragma unroll
      for (int sp = 1; sp < G2_KSPLIT_F; ++sp) {
        const float* gs = g0 + (size_t)sp * S_ * D_;
        ga0 += *reinterpret_cast<const f32x4*>(gs);
        ga1 += *reinterpret_cast<const f32x4*>(gs + 4);
      }
      bf16x8 ov;
#pragma unroll
      for (int j = 0; j < 8; ++j) {
        float2 z = buf[w][n0 + j];
        float sv = rbf((r == 0 ? z.x : z.y) * scale);
        float gpre = rbf(j < 4 ? ga0[j] : ga1[j - 4]);
        float g = rbf(gpre / (1.0f + __expf(-gpre)));
        float o = us2f((unsigned short)xv[j]) + rbf(sv * g);
        bf16 ob = __float2bfloat16(o);
        ov[j] = (short)*reinterpret_cast<unsigned short*>(&ob);
      }
      *reinterpret_cast<bf16x8*>(out + (size_t)row * D_ + n0) = ov;
    }
  }
}

__device__ __forceinline__ int lds_byte(int r, int c) {
  int st = (r >> 4) * 2 + (c >> 5), rr = r & 15, cc = c & 31, ob = rr * 64 + cc * 2;
  return st * 1024 + (ob ^ (((ob >> 9) & 1) << 5));
}
__device__ __forceinline__ void stage_rc(int b, int& R, int& C) {
  int st = b / 1024, sb = b % 1024, swz = sb ^ (((sb >> 9) & 1) << 5);
  R = (st >> 1) * 16 + swz / 64;
  C = (st & 1) * 32 + (swz % 64) / 2;
}
__device__ __forceinline__ int bswz(int row) { return ((row & 3) << 2) | ((row >> 2) & 3); }
__device__ __forceinline__ int tr_addr(int lane, int c, int ks, int t) {
  const int g = lane >> 4, q = (lane & 15) >> 2, p = lane & 3;
  const int row = 32 * ks + 8 * g + 4 * t + q;
  const int ch = 2 * c + (p >> 1);
  return 256 * row + 16 * (ch ^ bswz(row)) + 8 * (p & 1);
}
template <int OFF>
__device__ __forceinline__ s16x4 tr_read(unsigned addr) {
  s16x4 r;
  asm volatile("ds_read_b64_tr_b16 %0, %1 offset:%2" : "=v"(r) : "v"(addr), "i"(OFF) : "memory");
  return r;
}
template <int OFF>
__device__ __forceinline__ bf16x8 lds_read128(unsigned addr) {
  bf16x8 r;
  asm volatile("ds_read_b128 %0, %1 offset:%2" : "=v"(r) : "v"(addr), "i"(OFF) : "memory");
  return r;
}
struct BFrag { s16x4 lo, hi; };
template <int OFF>
__device__ __forceinline__ BFrag ldb_frag(unsigned a0, unsigned a1) {
  BFrag r;
  r.lo = tr_read<OFF>(a0);
  r.hi = tr_read<OFF>(a1);
  return r;
}
__device__ __forceinline__ bf16x8 mk8(const BFrag& f) {
  bf16x8 r;
  r[0] = f.lo[0]; r[1] = f.lo[1]; r[2] = f.lo[2]; r[3] = f.lo[3];
  r[4] = f.hi[0]; r[5] = f.hi[1]; r[6] = f.hi[2]; r[7] = f.hi[3];
  return r;
}

#define WAIT_V(n) asm volatile("s_waitcnt vmcnt(" #n ")" ::: "memory")
#define WAIT_L(n) asm volatile("s_waitcnt lgkmcnt(" #n ")" ::: "memory")
#define BAR __builtin_amdgcn_s_barrier()
#define SCHED __builtin_amdgcn_sched_barrier(0)

constexpr int G1_BM = 256, G1_BK = 64, G1_HALF = 128, G1_HT = G1_HALF * G1_BK, G1_SHM = 8 * G1_HT * 2;

constexpr int G1_NWG = (S_ / G1_BM) * (FF_ / G1_HALF);
constexpr int CF_NWG = 32;
constexpr int CF_SHM = 8 * CF_BUF * 8;
constexpr int G1_LAUNCH_SHM = CF_SHM > G1_SHM ? CF_SHM : G1_SHM;

__global__ __launch_bounds__(512, 2) void k_gemm1(const bf16* __restrict__ A, const bf16* __restrict__ Wg,
                                                   const bf16* __restrict__ Wu, bf16* __restrict__ H,
                                                   float2* __restrict__ T, const unsigned* __restrict__ Wt) {
  extern __shared__ __attribute__((aligned(16))) bf16 shm[];
  if (blockIdx.x >= G1_NWG) {
    const int lane = threadIdx.x & 63, w = threadIdx.x >> 6;
    v2f* buf = reinterpret_cast<v2f*>(shm) + w * CF_BUF;
    v2f tA0 = vtwid(lane, 1.0f / 1024.0f), tA1 = vtwid(lane + 64, 1.0f / 1024.0f);
    v2f tB = vtwid(lane & 7, 1.0f / 64.0f);
    const int c0 = (blockIdx.x - G1_NWG) * 16 + w;
    const int ncol = (c0 == (CF_NWG - 1) * 16) ? 3 : 2;
    v2f* Tv = reinterpret_cast<v2f*>(T);
#pragma unroll 1
    for (int c = 0; c < ncol; ++c) {
      asm volatile("" : "+v"(tA0), "+v"(tA1), "+v"(tB));
      const int col = c0 + 8 * c;
      colfft_one(Tv + (size_t)col * S_, Wt + (size_t)col * S_, buf, lane, tA0, tA1, tB);
    }
    return;
  }
  constexpr int K = D_, NW = FF_;
#define SA(b, h) (shm + ((b) * 2 + (h)) * G1_HT)
#define SB(b, h) (shm + (4 + (b) * 2 + (h)) * G1_HT)
#define STAGE_A(T, P, br, kt)                                                                   \
  do {                                                                                          \
    long _g = (long)(br) * K + (long)(kt) * G1_BK;                                              \
    _Pragma("unroll") for (int _i = 0; _i < 2; ++_i) {                                                            \
      int _b = (T) * 16 + _i * 8192;                                                            \
      int _r, _c;                                                                               \
      stage_rc(_b, _r, _c);                                                                     \
      __builtin_amdgcn_global_load_lds((const unsigned*)(A + _g + (long)_r * K + _c),           \
                                       (unsigned*)((char*)(P) + _b), 16, 0, 0);                 \
    }                                                                                           \
  } while (0)
#define STAGE_B(T, P, W, kt)                                                                    \
  do {                                                                                          \
    _Pragma("unroll") for (int _i = 0; _i < 2; ++_i) {                                                            \
      int _b = (T) * 16 + _i * 8192;                                                            \
      int _row = _b >> 8, _ch = ((_b >> 4) & 15) ^ bswz(_row);                                  \
      __builtin_amdgcn_global_load_lds(                                                         \
          (const unsigned*)(W + (long)((kt) * G1_BK + _row) * NW + bcol + _ch * 8),             \
          (unsigned*)((char*)(P) + _b), 16, 0, 0);                                              \
    }                                                                                           \
  } while (0)
#define LDA(dst, b, h)                                                                          \
  _Pragma("unroll") for (int m = 0; m < 4; ++m)                                                 \
    _Pragma("unroll") for (int k = 0; k < 2; ++k)                                               \
  dst[m][k] = *reinterpret_cast<const bf16x8*>((char*)SA(b, h) + lds_byte(wr * 64 + m * 16 + fr, k * 32 + fq * 8))
#define LDB(dst, b, h)                                                                          \
  do {                                                                                          \
    dst[0][0] = ldb_frag<((b) * 2 + (h)) * 16384>(tb[0][0], tb[0][1]);                          \
    dst[0][1] = ldb_frag<((b) * 2 + (h)) * 16384 + 8192>(tb[0][0], tb[0][1]);                   \
    dst[1][0] = ldb_frag<((b) * 2 + (h)) * 16384>(tb[1][0], tb[1][1]);                          \
    dst[1][1] = ldb_frag<((b) * 2 + (h)) * 16384 + 8192>(tb[1][0], tb[1][1]);                   \
  } while (0)
#define MMA(ai, bj, At, Bt)                                                                     \
  do {                                                                                          \
    __builtin_amdgcn_s_setprio(1);                                                              \
    _Pragma("unroll") for (int m = 0; m < 4; ++m)                                               \
      _Pragma("unroll") for (int n = 0; n < 2; ++n)                                             \
        _Pragma("unroll") for (int k = 0; k < 2; ++k)                                           \
          acc[ai][bj][m][n] =                                                                   \
              __builtin_amdgcn_mfma_f32_16x16x32_bf16(At[m][k], mk8(Bt[n][k]), acc[ai][bj][m][n], 0, 0, 0); \
    __builtin_amdgcn_s_setprio(0);                                                              \
  } while (0)

  constexpr int nM = S_ / G1_BM, nN = FF_ / G1_HALF, nwg = nM * nN;
  int wgid = blockIdx.x;
  {
    int q = nwg / 8, r = nwg % 8, xcd = wgid % 8, off = wgid / 8;
    wgid = (xcd < r ? xcd * (q + 1) : r * (q + 1) + (xcd - r) * q) + off;
  }
  constexpr int WGM = 8;
  int nig = WGM * nN, gid = wgid / nig, fm = gid * WGM, gsz = min(nM - fm, WGM);
  int pm = fm + ((wgid % nig) % gsz), pn = (wgid % nig) / gsz, brow = pm * G1_BM, bcol = pn * G1_HALF;
  int wid = threadIdx.x >> 6, lane = threadIdx.x & 63, wr = wid >> 2, wc = wid & 3, fr = lane & 15, fq = lane >> 4;
  f32x4 acc[2][2][4][2] = {};
  bf16x8 At[4][2];
  BFrag B0[2][2], B1[2][2];
  unsigned tb[2][2];
  {
    const unsigned sbase = (unsigned)(size_t)SB(0, 0);
    tb[0][0] = sbase + tr_addr(lane, wc * 2, 0, 0);
    tb[0][1] = sbase + tr_addr(lane, wc * 2, 0, 1);
    tb[1][0] = sbase + tr_addr(lane, wc * 2 + 1, 0, 0);
    tb[1][1] = sbase + tr_addr(lane, wc * 2 + 1, 0, 1);
  }
  constexpr int nt = K / G1_BK;
  STAGE_B(threadIdx.x, SB(0, 0), Wg, 0);
  STAGE_A(threadIdx.x, SA(0, 0), brow, 0);
  STAGE_B(threadIdx.x, SB(0, 1), Wu, 0);
  STAGE_A(threadIdx.x, SA(0, 1), brow + G1_HALF, 0);
  if (wr == 1) BAR;
  WAIT_V(4);
  BAR;
  STAGE_B(threadIdx.x, SB(1, 0), Wg, 1);
  STAGE_A(threadIdx.x, SA(1, 0), brow, 1);
  STAGE_B(threadIdx.x, SB(1, 1), Wu, 1);
  WAIT_V(6);
  BAR;
  for (int t = 0; t < nt - 2; t += 2) {
    LDB(B0, 0, 0); SCHED; LDA(At, 0, 0); STAGE_A(threadIdx.x, SA(1, 1), brow + G1_HALF, t + 1);
    WAIT_L(8); BAR; WAIT_L(0); SCHED; MMA(0, 0, At, B0); BAR; SCHED;
    LDB(B1, 0, 1); STAGE_B(threadIdx.x, SB(0, 0), Wg, t + 2);
    BAR; WAIT_L(0); SCHED; MMA(0, 1, At, B1); BAR;
    LDA(At, 0, 1); STAGE_A(threadIdx.x, SA(0, 0), brow, t + 2);
    BAR; WAIT_L(0); SCHED; MMA(1, 0, At, B0); BAR; SCHED;
    STAGE_B(threadIdx.x, SB(0, 1), Wu, t + 2);
    WAIT_V(6); BAR; MMA(1, 1, At, B1); BAR;
    LDB(B0, 1, 0); SCHED; LDA(At, 1, 0); STAGE_A(threadIdx.x, SA(0, 1), brow + G1_HALF, t + 2);
    WAIT_L(8); BAR; WAIT_L(0); SCHED; MMA(0, 0, At, B0); BAR; SCHED;
    LDB(B1, 1, 1); STAGE_B(threadIdx.x, SB(1, 0), Wg, t + 3);
    BAR; WAIT_L(0); SCHED; MMA(0, 1, At, B1); BAR;
    LDA(At, 1, 1); STAGE_A(threadIdx.x, SA(1, 0), brow, t + 3);
    BAR; WAIT_L(0); SCHED; MMA(1, 0, At, B0); BAR; SCHED;
    STAGE_B(threadIdx.x, SB(1, 1), Wu, t + 3);
    WAIT_V(6); BAR; MMA(1, 1, At, B1); BAR;
  }
  int tz = threadIdx.x;
  asm volatile("" : "+v"(tz));
  wid = tz >> 6; lane = tz & 63; wr = wid >> 2; wc = wid & 3; fr = lane & 15; fq = lane >> 4;
  {
    LDB(B0, 0, 0); WAIT_V(0); LDA(At, 0, 0); STAGE_A(tz, SA(1, 1), brow + G1_HALF, nt - 1);
    BAR; WAIT_L(0); SCHED; MMA(0, 0, At, B0); BAR;
    LDB(B1, 0, 1); BAR; WAIT_L(0); SCHED; MMA(0, 1, At, B1); BAR;
    LDA(At, 0, 1); WAIT_V(4); BAR; WAIT_L(0); SCHED; MMA(1, 0, At, B0); MMA(1, 1, At, B1); BAR;
  }
  {
    LDB(B0, 1, 0); LDA(At, 1, 0); WAIT_V(2); BAR; WAIT_L(0); SCHED; MMA(0, 0, At, B0); BAR;
    LDB(B1, 1, 1); WAIT_V(0); BAR; WAIT_L(0); SCHED; MMA(0, 1, At, B1); BAR;
    LDA(At, 1, 1); BAR; WAIT_L(0); SCHED; MMA(1, 0, At, B0); MMA(1, 1, At, B1); BAR;
  }
  if (wr == 0) BAR;
#pragma unroll
  for (int ai = 0; ai < 2; ++ai)
#pragma unroll
    for (int m = 0; m < 4; ++m)
#pragma unroll
      for (int n = 0; n < 2; ++n)
#pragma unroll
        for (int j = 0; j < 4; ++j) {
          float g = rbf(acc[ai][0][m][n][j]);
          float u = rbf(acc[ai][1][m][n][j]);
          float inner = 0.7978845608028654f * (g + 0.044715f * g * g * g);
          float ge = rbf(g * __builtin_amdgcn_rcpf(1.0f + __expf(-2.0f * inner)));
          H[(size_t)(brow + ai * G1_HALF + wr * 64 + m * 16 + fq * 4 + j) * FF_ + (bcol + wc * 32 + n * 16 + fr)] =
              __float2bfloat16(ge * u);
        }
#undef SA
#undef SB
#undef STAGE_A
#undef STAGE_B
#undef LDA
#undef LDB
#undef MMA
}

constexpr int G2_KSPLIT = 4, G2_KLEN = FF_ / G2_KSPLIT;
constexpr int G2_STG = 49152, G2_SHM = 3 * G2_STG;
__global__ __launch_bounds__(512, 2) void k_gemm2(const bf16* __restrict__ A, const bf16* __restrict__ W,
                                                  float* __restrict__ Cp) {
  extern __shared__ __attribute__((aligned(16))) bf16 shm[];
  constexpr int K = FF_, NW = D_;
  const int tid = threadIdx.x, lane = tid & 63, wid = tid >> 6, wr = wid >> 1, wc = wid & 1;
  const int fr = lane & 15, fq = lane >> 4;
  const int bid = blockIdx.x;
  const int pm = bid & 7, idx = bid >> 3;
  const int pn = idx & 7, ks = idx >> 3;
  const int brow = pm * 256, bcol = pn * 128, k0 = ks * G2_KLEN;
  f32x4 acc[4][4] = {};
  unsigned tb[4][2];
  const unsigned ta = (unsigned)(size_t)shm + (wr >> 1) * 16384 + lds_byte((wr & 1) * 64 + fr, fq * 8);
  {
    const unsigned sbase = (unsigned)(size_t)shm + 32768;
#pragma unroll
    for (int n = 0; n < 4; ++n) {
      tb[n][0] = sbase + tr_addr(lane, wc * 4 + n, 0, 0);
      tb[n][1] = sbase + tr_addr(lane, wc * 4 + n, 0, 1);
    }
  }
  constexpr int nt = G2_KLEN / 64;
#define G2_STAGE(sbo, kt)                                                                       \
  do {                                                                                          \
    _Pragma("unroll") for (int _i = 0; _i < 4; ++_i) {                                          \
      int _b = tid * 16 + (_i & 1) * 8192;                                                      \
      int _r, _c;                                                                               \
      stage_rc(_b, _r, _c);                                                                     \
      __builtin_amdgcn_global_load_lds(                                                         \
          (const unsigned*)(A + (long)(brow + (_i >> 1) * 128 + _r) * K + k0 + (kt) * 64 + _c), \
          (unsigned*)((char*)shm + (sbo) + (_i >> 1) * 16384 + _b), 16, 0, 0);                  \
    }                                                                                           \
    _Pragma("unroll") for (int _i = 0; _i < 2; ++_i) {                                          \
      int _b = tid * 16 + _i * 8192;                                                            \
      int _row = _b >> 8, _ch = ((_b >> 4) & 15) ^ bswz(_row);                                  \
      __builtin_amdgcn_global_load_lds(                                                         \
          (const unsigned*)(W + (long)(k0 + (kt) * 64 + _row) * NW + bcol + _ch * 8),           \
          (unsigned*)((char*)shm + (sbo) + 32768 + _b), 16, 0, 0);                              \
    }                                                                                           \
  } while (0)
  bf16x8 af[2][4];
  BFrag bfr[2][4];
#define G2_FRAGS(set, sbo, KS)                                                                  \
  do {                                                                                          \
    af[set][0] = lds_read128<(KS) * 1024>(ta + (sbo));                                          \
    af[set][1] = lds_read128<(KS) * 1024 + 2048>(ta + (sbo));                                   \
    af[set][2] = lds_read128<(KS) * 1024 + 4096>(ta + (sbo));                                   \
    af[set][3] = lds_read128<(KS) * 1024 + 6144>(ta + (sbo));                                   \
    _Pragma("unroll") for (int n = 0; n < 4; ++n)                                               \
      bfr[set][n] = ldb_frag<(KS) * 8192>(tb[n][0] + (sbo), tb[n][1] + (sbo));                  \
  } while (0)
#define G2_MMA(set)                                                                             \
  do {                                                                                          \
    _Pragma("unroll") for (int m = 0; m < 4; ++m)                                               \
      _Pragma("unroll") for (int n = 0; n < 4; ++n)                                             \
        acc[m][n] = __builtin_amdgcn_mfma_f32_16x16x32_bf16(af[set][m], mk8(bfr[set][n]), acc[m][n], 0, 0, 0); \
  } while (0)
  G2_STAGE(0, 0);
  G2_STAGE(G2_STG, 1);
  WAIT_V(6);
  BAR;
  G2_FRAGS(0, 0, 0);
  unsigned cur = 0, nxt = G2_STG, nx2 = 2 * G2_STG;
  for (int t = 0; t < nt - 2; ++t) {
    G2_FRAGS(1, cur, 1);
    WAIT_L(12);
    SCHED;
    G2_MMA(0);
    SCHED;
    WAIT_L(0);
    WAIT_V(0);
    BAR;
    G2_STAGE(nx2, t + 2);
    G2_FRAGS(0, nxt, 0);
    SCHED;
    G2_MMA(1);
    SCHED;
    const unsigned tmp = cur;
    cur = nxt; nxt = nx2; nx2 = tmp;
  }
  G2_FRAGS(1, cur, 1);
  WAIT_L(12);
  SCHED;
  G2_MMA(0);
  SCHED;
  WAIT_L(0);
  WAIT_V(0);
  BAR;
  G2_FRAGS(0, nxt, 0);
  SCHED;
  G2_MMA(1);
  SCHED;
  G2_FRAGS(1, nxt, 1);
  WAIT_L(12);
  SCHED;
  G2_MMA(0);
  SCHED;
  WAIT_L(0);
  SCHED;
  G2_MMA(1);
  float* C = Cp + (size_t)ks * S_ * D_;
#pragma unroll
  for (int m = 0; m < 4; ++m)
#pragma unroll
    for (int n = 0; n < 4; ++n)
#pragma unroll
      for (int j = 0; j < 4; ++j)
        C[(size_t)(brow + wr * 64 + m * 16 + fq * 4 + j) * D_ + bcol + wc * 64 + n * 16 + fr] = acc[m][n][j];
#undef G2_STAGE
#undef G2_FRAGS
#undef G2_MMA
}

extern "C" void kernel_launch(void* const* d_in, const int* in_sizes, int n_in, void* d_out, int out_size,
                              void* d_ws, size_t ws_size, hipStream_t stream) {
  const bf16* x = (const bf16*)d_in[0];
  const bf16* wr = (const bf16*)d_in[1];
  const bf16* wi = (const bf16*)d_in[2];
  const bf16* wg = (const bf16*)d_in[4];
  const bf16* wu = (const bf16*)d_in[5];
  const bf16* wd = (const bf16*)d_in[6];
  bf16* out = (bf16*)d_out;
  char* ws = (char*)d_ws;
  bf16* xn = (bf16*)ws;
  float2* T = (float2*)(ws + (size_t)8 * 1024 * 1024);
  bf16* h = (bf16*)(ws + (size_t)24 * 1024 * 1024);
  unsigned* Wt = (unsigned*)(ws + (size_t)40 * 1024 * 1024);
  float* gp = (float*)(ws + (size_t)48 * 1024 * 1024);

  (void)hipFuncSetAttribute((const void*)k_gemm1, hipFuncAttributeMaxDynamicSharedMemorySize, G1_LAUNCH_SHM);
  k_ln_fft<<<S_ / 8, 256, 0, stream>>>(x, xn, T, (const unsigned short*)wr, (const unsigned short*)wi, Wt);
  k_gemm1<<<G1_NWG + CF_NWG, 512, G1_LAUNCH_SHM, stream>>>(xn, wg, wu, h, T, Wt);
  (void)hipFuncSetAttribute((const void*)k_gemm2, hipFuncAttributeMaxDynamicSharedMemorySize, G2_SHM);
  k_gemm2<<<(S_ / 256) * (D_ / 128) * G2_KSPLIT, 512, G2_SHM, stream>>>(h, wd, gp);
  k_final<<<S_ / 8, 256, 0, stream>>>(T, x, gp, out);
}
```
